```python
import torch

def _softmax(x, axis=-1):
    return torch.softmax(x, dim=axis)
def _gelu(x):
    return torch.nn.functional.gelu(x, approximate="tanh")
def _silu(x):
    return torch.nn.functional.silu(x)
def _sigmoid(x):
    return torch.sigmoid(x)
def _layernorm(x, axis=-1, eps=1e-5):
    m = x.mean(dim=axis, keepdim=True)
    v = x.var(dim=axis, keepdim=True, unbiased=False)
    return (x - m) / torch.sqrt(v + eps)
def _scatter_add(tgt, idx, src, axis=0):
    return tgt.clone().index_add_(axis, idx, src)
def _segment_sum(src, seg, n):
    out = torch.zeros((n,) + src.shape[1:], dtype=src.dtype)
    return out.index_add_(0, seg, src)
def _pad(x, pw):
    flat = tuple(v for pair in reversed(pw) for v in pair)
    return torch.nn.functional.pad(x, flat)

kernel_name = 'parallel_sum__mha_block_cached__swiglu_ffn__decode'

def setup_inputs():

    _w = (lambda a: (lambda t: t.to(torch.bfloat16) if t.is_floating_point() else t)(torch.as_tensor(a)))
    def _A_setup():
        B, Sq, D = 4, 1, 512
        H, Sk, Dh = 32, 8192, 128
        return {
            "x": _w((lambda *s: torch.randn(*s, dtype=torch.bfloat16) * 0.05)(B, Sq, D)),
            "k_cache": _w((lambda *s: torch.randn(*s, dtype=torch.bfloat16) * 3.2)(B, H, Sk, Dh)),
            "v_cache": _w((lambda *s: torch.randn(*s, dtype=torch.bfloat16) * 1.6)(B, H, Sk, Dh)),
            "wq": _w((lambda *s: torch.randn(*s, dtype=torch.bfloat16) * 3.2)(D, H * Dh) * (D ** -0.5)),
            "wo": _w((lambda *s: torch.randn(*s, dtype=torch.bfloat16) * 1.6)(H * Dh, D) * ((H * Dh) ** -0.5)),
        }
    _A_in = _A_setup()

    def _B_setup():
        B, S, D, F = 4, 1, 512, 1792
        return {
            "x": _w((lambda *s: torch.randn(*s, dtype=torch.bfloat16) * 0.05)(B, S, D)),
            "w_gate": _w((lambda *s: torch.randn(*s, dtype=torch.bfloat16) * 0.05)(D, F)),
            "w_up": _w((lambda *s: torch.randn(*s, dtype=torch.bfloat16) * 0.05)(D, F)),
            "w_down": _w((lambda *s: torch.randn(*s, dtype=torch.bfloat16) * 0.05)(F, D)),
        }
    _B_in = _B_setup()

    return {
        "A_x": _A_in["x"],
        "A_k_cache": _A_in["k_cache"],
        "A_v_cache": _A_in["v_cache"],
        "A_wq": _A_in["wq"],
        "A_wo": _A_in["wo"],
        "B_x": _B_in["x"],
        "B_w_gate": _B_in["w_gate"],
        "B_w_up": _B_in["w_up"],
        "B_w_down": _B_in["w_down"],
    }

def reference(A_x, A_k_cache, A_v_cache, A_wq, A_wo, B_x, B_w_gate, B_w_up, B_w_down):
    def _A_ref(x, k_cache, v_cache, wq, wo):
        B, Sq, D = x.shape
        H, Dh = 32, 128
        q = (lambda x, *d: x.permute(*d))((x @ wq).reshape(B, Sq, H, Dh), 0, 2, 1, 3)
        scores = torch.einsum("bhqd,bhkd->bhqk", q, k_cache) * (Dh ** -0.5)
        p = _softmax(scores, axis=-1)
        o = torch.einsum("bhqk,bhkd->bhqd", p, v_cache)
        return (lambda x, *d: x.permute(*d))(o, 0, 2, 1, 3).reshape(B, Sq, H * Dh) @ wo

    def _B_ref(x, w_gate, w_up, w_down):
        g = _silu(x @ w_gate)
        u = x @ w_up
        return (g * u) @ w_down

    x = A_x
    xn = _layernorm(x, axis=-1)
    return x + _A_ref(xn, A_k_cache, A_v_cache, A_wq, A_wo) + _B_ref(xn, B_w_gate, B_w_up, B_w_down)
```

```cpp
#include <hip/hip_runtime.h>
#include <hip/hip_bf16.h>
#include <stdint.h>

typedef unsigned short u16;
typedef unsigned v4u __attribute__((ext_vector_type(4)));
typedef unsigned v2u __attribute__((ext_vector_type(2)));
typedef float f32x4 __attribute__((ext_vector_type(4)));
typedef float f32x2 __attribute__((ext_vector_type(2)));
typedef __bf16 bf16x2 __attribute__((ext_vector_type(2)));
typedef __amdgpu_buffer_rsrc_t rsrc_t;

namespace {

constexpr int NB = 4, DM = 512, NH = 32, SK = 8192, DH = 128, FF = 1792;
constexpr float SCALE = 0.08838834764831845f;
constexpr float LOG2E = 1.4426950408889634f;
constexpr int AUX_NT = 2, AUX_SC1 = 16;
constexpr unsigned TAG_STEP = 0x9E3779B9u;
constexpr unsigned SPIN_CAP = 1u << 18;

constexpr unsigned WS_EPOCH = 0;
constexpr unsigned WS_ML = 4096;
constexpr unsigned WS_Y = WS_ML + 128 * 128;
constexpr unsigned WS_END = WS_Y + 4 * 64 * 64 * 64;

constexpr int L_SC = 0;
constexpr int L_XN = L_SC + 4096;
constexpr int L_QP = L_XN + 1024;
constexpr int L_OP = L_QP + 512;
constexpr int L_HB = L_OP + 512;
constexpr int L_MX = L_HB + 16;
constexpr int L_TAG = L_MX + 4;
constexpr int L_END = L_TAG + 4;

__device__ __forceinline__ rsrc_t mk_rsrc(const void* p, unsigned bytes) {
  return __builtin_amdgcn_make_buffer_rsrc(const_cast<void*>(p), 0, bytes, 0x00020000);
}
template <int AUX> __device__ __forceinline__ v4u ld16(rsrc_t r, unsigned voff, unsigned soff) {
  return __builtin_amdgcn_raw_buffer_load_b128(r, voff, soff, AUX);
}
__device__ __forceinline__ float dot2(unsigned a, unsigned b, float acc) {
  return __builtin_amdgcn_fdot2_f32_bf16(__builtin_bit_cast(bf16x2, a), __builtin_bit_cast(bf16x2, b), acc, false);
}
__device__ __forceinline__ float bflo(unsigned w) { return __uint_as_float(w << 16); }
__device__ __forceinline__ float bfhi(unsigned w) { return __uint_as_float(w & 0xffff0000u); }
__device__ __forceinline__ unsigned pk_bf16(float lo, float hi) {
  bf16x2 v = {(__bf16)lo, (__bf16)hi};
  return __builtin_bit_cast(unsigned, v);
}
__device__ __forceinline__ float rbf(float x) { return (float)(__bf16)x; }
template <int CTRL> __device__ __forceinline__ float dpp(float x) {
  return __builtin_bit_cast(float, __builtin_amdgcn_mov_dpp(__builtin_bit_cast(int, x), CTRL, 0xf, 0xf, true));
}
template <int CTRL> __device__ __forceinline__ unsigned dppu(unsigned x) {
  return (unsigned)__builtin_amdgcn_mov_dpp((int)x, CTRL, 0xf, 0xf, true);
}
constexpr int XOR1 = 0xB1, XOR2 = 0x4E, XOR7 = 0x141, XOR8 = 0x128, BCAST = 0x150;
__device__ __forceinline__ float swap32_add(float a, float b) {
  auto t = __builtin_amdgcn_permlane32_swap(__float_as_uint(a), __float_as_uint(b), false, false);
  return __uint_as_float(t[0]) + __uint_as_float(t[1]);
}
__device__ __forceinline__ float swap16_add(float a, float b) {
  auto t = __builtin_amdgcn_permlane16_swap(__float_as_uint(a), __float_as_uint(b), false, false);
  return __uint_as_float(t[0]) + __uint_as_float(t[1]);
}
__device__ __forceinline__ float wave_sum(float x) {
  x += dpp<XOR1>(x); x += dpp<XOR2>(x); x += dpp<XOR7>(x); x += dpp<XOR8>(x);
  x = swap16_add(x, x); x = swap32_add(x, x);
  return x;
}
__device__ __forceinline__ float wave_max(float x) {
  x = fmaxf(x, dpp<XOR1>(x)); x = fmaxf(x, dpp<XOR2>(x)); x = fmaxf(x, dpp<XOR7>(x)); x = fmaxf(x, dpp<XOR8>(x));
  auto s = __builtin_amdgcn_permlane16_swap(__float_as_uint(x), __float_as_uint(x), false, false);
  x = fmaxf(__uint_as_float(s[0]), __uint_as_float(s[1]));
  auto t = __builtin_amdgcn_permlane32_swap(__float_as_uint(x), __float_as_uint(x), false, false);
  return fmaxf(__uint_as_float(t[0]), __uint_as_float(t[1]));
}
__device__ __forceinline__ void rows_reduce8(const float (&a)[8], float& o0, float& o1) {
  const float t0 = swap32_add(a[0], a[4]), t1 = swap32_add(a[1], a[5]), t2 = swap32_add(a[2], a[6]), t3 = swap32_add(a[3], a[7]);
  o0 = swap16_add(t0, t2); o1 = swap16_add(t1, t3);
}
__device__ __forceinline__ void wg_barrier() { asm volatile("s_waitcnt lgkmcnt(0)\n\ts_barrier" ::: "memory"); }
#define SCHED_FENCE() __builtin_amdgcn_sched_barrier(0)
#define PIN8(a) asm volatile("" : "+v"((a)[0]), "+v"((a)[1]), "+v"((a)[2]), "+v"((a)[3]), "+v"((a)[4]), "+v"((a)[5]), "+v"((a)[6]), "+v"((a)[7]) :: "memory")
#define MEM_FENCE() asm volatile("" ::: "memory")
__device__ __forceinline__ unsigned perm_lo(unsigned a, unsigned b) { return __builtin_amdgcn_perm(b, a, 0x05040100u); }
__device__ __forceinline__ unsigned perm_hi(unsigned a, unsigned b) { return __builtin_amdgcn_perm(b, a, 0x07060302u); }
__device__ __forceinline__ void gemv_pair(float (&acc)[8], const v4u& ra, const v4u& rb, unsigned xp) {
#pragma unroll
  for (int k = 0; k < 4; ++k) {
    acc[2 * k] = dot2(perm_lo(ra[k], rb[k]), xp, acc[2 * k]);
    acc[2 * k + 1] = dot2(perm_hi(ra[k], rb[k]), xp, acc[2 * k + 1]);
  }
}

template <bool REFILL>
__device__ __forceinline__ float k_group(v4u (&A)[8], v4u (&Bv)[8], const v4u& q, int c, rsrc_t rn, unsigned voff, unsigned offA, unsigned offB) {
  float x[16];
#pragma unroll
  for (int i = 0; i < 8; ++i) {
    float d = dot2(A[i][0], q[0], 0.f); d = dot2(A[i][1], q[1], d); d = dot2(A[i][2], q[2], d); x[i] = dot2(A[i][3], q[3], d);
  }
  PIN8(x);
  SCHED_FENCE();
  if (REFILL) {
#pragma unroll
    for (int i = 0; i < 8; ++i) A[i] = ld16<AUX_NT>(rn, voff + (i & 3) * 1024, offA + (i >> 2) * 4096);
  }
  SCHED_FENCE();
#pragma unroll
  for (int i = 0; i < 8; ++i) {
    float d = dot2(Bv[i][0], q[0], 0.f); d = dot2(Bv[i][1], q[1], d); d = dot2(Bv[i][2], q[2], d); x[8 + i] = dot2(Bv[i][3], q[3], d);
  }
  PIN8(x + 8);
  SCHED_FENCE();
  if (REFILL) {
#pragma unroll
    for (int i = 0; i < 8; ++i) Bv[i] = ld16<AUX_NT>(rn, voff + (i & 3) * 1024, offB + (i >> 2) * 4096);
  }
  SCHED_FENCE();
#define TR_STEP(HALF, CTRL, BIT)                                                                 \
  _Pragma("unroll") for (int hh_ = 0; hh_ < (HALF); ++hh_) {                                     \
    const float lo_ = x[hh_], hi_ = x[hh_ + (HALF)];                                             \
    const float t1_ = lo_ + dpp<CTRL>(lo_), t2_ = hi_ + dpp<CTRL>(hi_);                          \
    x[hh_] = (c & (BIT)) ? t2_ : t1_;                                                            \
  }
  TR_STEP(8, XOR8, 8) TR_STEP(4, XOR7, 4) TR_STEP(2, XOR2, 2) TR_STEP(1, XOR1, 1)
#undef TR_STEP
  return rbf(rbf(x[0]) * SCALE);
}

template <int I> __device__ __forceinline__ void pv_one(float (&acc)[8], const v4u& V, float p) {
  const float pb = dpp<BCAST + I>(p);
#pragma unroll
  for (int k = 0; k < 4; ++k) {
    acc[2 * k] = fmaf(pb, bflo(V[k]), acc[2 * k]);
    acc[2 * k + 1] = fmaf(pb, bfhi(V[k]), acc[2 * k + 1]);
  }
}

__global__ void __launch_bounds__(512, 2)
fused_decode_kernel(const u16* __restrict__ x, const u16* __restrict__ kc, const u16* __restrict__ vc, const u16* __restrict__ wq,
                    const u16* __restrict__ wo, const u16* __restrict__ wg, const u16* __restrict__ wu, const u16* __restrict__ wd,
                    u16* __restrict__ out, unsigned char* __restrict__ ws) {
  __shared__ __attribute__((aligned(16))) unsigned lds[L_END];
  float* ldsf = reinterpret_cast<float*>(lds);
  const int tid = threadIdx.x, lane = tid & 63, wave = __builtin_amdgcn_readfirstlane(tid >> 6);
  const int r = lane >> 4, c = lane & 15;
  const int bid = blockIdx.x, xg = bid & 7, idx = bid >> 3;
  const int hh = idx >> 3, h = xg + 8 * hh, b = (idx >> 1) & 3, s = idx & 1;
  const int bh = b * NH + h, u = h * 2 + s;
  const rsrc_t rws = mk_rsrc(ws, WS_END);

  if (wave < 4) {
    const size_t kvoff = ((size_t)bh * SK + (size_t)s * (SK / 2)) * DH;
    const rsrc_t rk = mk_rsrc(kc + kvoff, 1u << 20), rv = mk_rsrc(vc + kvoff, 1u << 20);
    const unsigned voff = lane * 16 + wave * 8192;
    const int rot = idx;
#define HG_OFF(hg) ((unsigned)(((hg) + rot) & 31) << 15)
    v4u PA[8], PB[8], A[8], Bv[8];
#pragma unroll
    for (int i = 0; i < 8; ++i) PA[i] = ld16<AUX_NT>(rk, voff + (i & 3) * 1024, HG_OFF(0) + (i >> 2) * 4096);
    SCHED_FENCE();
#pragma unroll
    for (int i = 0; i < 8; ++i) PB[i] = ld16<AUX_NT>(rk, voff + (i & 3) * 1024, HG_OFF(1) + (i >> 2) * 4096);
    SCHED_FENCE();
#pragma unroll
    for (int i = 0; i < 8; ++i) A[i] = ld16<AUX_NT>(rk, voff + (i & 3) * 1024, HG_OFF(2) + (i >> 2) * 4096);
    SCHED_FENCE();
#pragma unroll
    for (int i = 0; i < 8; ++i) Bv[i] = ld16<AUX_NT>(rk, voff + (i & 3) * 1024, HG_OFF(3) + (i >> 2) * 4096);
    SCHED_FENCE();
    wg_barrier();
    v4u q;
    {
      float qs[8];
#pragma unroll
      for (int k = 0; k < 8; ++k) qs[k] = 0.f;
#pragma unroll
      for (int w = 0; w < 4; ++w) {
        const f32x4 a0 = *reinterpret_cast<const f32x4*>(ldsf + L_QP + w * 128 + 8 * c);
        const f32x4 a1 = *reinterpret_cast<const f32x4*>(ldsf + L_QP + w * 128 + 8 * c + 4);
#pragma unroll
        for (int k = 0; k < 4; ++k) { qs[k] += a0[k]; qs[4 + k] += a1[k]; }
      }
#pragma unroll
      for (int k = 0; k < 4; ++k) q[k] = pk_bf16(qs[2 * k], qs[2 * k + 1]);
    }
    const unsigned tag = lds[L_TAG];
    float* sc = ldsf + L_SC + wave * 1024 + lane;
    float mx = k_group<false>(PA, PB, q, c, rk, voff, 0, 0);
    sc[0] = mx;
    for (int g = 1; g < 16; ++g) {
      const rsrc_t rn = g < 15 ? rk : rv;
      const float sv = k_group<true>(A, Bv, q, c, rn, voff, HG_OFF(2 * g + 2), HG_OFF(2 * g + 3));
      sc[g * 64] = sv;
      mx = fmaxf(mx, sv);
    }
    mx = wave_max(mx);
    if (lane == 0) ldsf[L_MX + wave] = mx;
    wg_barrier();
    {
      const f32x4 m4 = *reinterpret_cast<const f32x4*>(ldsf + L_MX);
      mx = fmaxf(fmaxf(m4[0], m4[1]), fmaxf(m4[2], m4[3]));
    }
    float l = 0.f;
#pragma unroll
    for (int g = 0; g < 16; ++g) {
      const float p = __builtin_amdgcn_exp2f((sc[g * 64] - mx) * LOG2E);
      sc[g * 64] = p;
      l += p;
    }
    l = wave_sum(l);
    {
      const v4u gr = {__float_as_uint(mx), tag, __float_as_uint(l), tag};
      const unsigned o = lane == 0 ? WS_ML + (unsigned)((bh * 2 + s) * 4 + wave) * 16u : 0x7ffffff0u;
      __builtin_amdgcn_raw_buffer_store_b128(gr, rws, o, 0, AUX_SC1);
    }
    float acc[8];
#pragma unroll
    for (int k = 0; k < 8; ++k) acc[k] = 0.f;
    for (int g = 0; g < 16; ++g) {
      const float p = sc[g * 64];
      const unsigned offA = g < 15 ? HG_OFF(2 * g + 2) : (1u << 20), offB = g < 15 ? HG_OFF(2 * g + 3) : (1u << 20);
      pv_one<0>(acc, A[0], p); pv_one<1>(acc, A[1], p); pv_one<2>(acc, A[2], p); pv_one<3>(acc, A[3], p);
      pv_one<4>(acc, A[4], p); pv_one<5>(acc, A[5], p); pv_one<6>(acc, A[6], p); pv_one<7>(acc, A[7], p);
      PIN8(acc);
      SCHED_FENCE();
#pragma unroll
      for (int i = 0; i < 8; ++i) A[i] = ld16<AUX_NT>(rv, voff + (i & 3) * 1024, offA + (i >> 2) * 4096);
      SCHED_FENCE();
      pv_one<8>(acc, Bv[0], p); pv_one<9>(acc, Bv[1], p); pv_one<10>(acc, Bv[2], p); pv_one<11>(acc, Bv[3], p);
      pv_one<12>(acc, Bv[4], p); pv_one<13>(acc, Bv[5], p); pv_one<14>(acc, Bv[6], p); pv_one<15>(acc, Bv[7], p);
      PIN8(acc);
      SCHED_FENCE();
#pragma unroll
      for (int i = 0; i < 8; ++i) Bv[i] = ld16<AUX_NT>(rv, voff + (i & 3) * 1024, offB + (i >> 2) * 4096);
      SCHED_FENCE();
    }
    {
      float o0, o1;
      rows_reduce8(acc, o0, o1);
      *reinterpret_cast<f32x2*>(ldsf + L_OP + wave * 128 + 8 * c + 2 * r) = f32x2{o0, o1};
    }
    wg_barrier();
    if (wave != 0) return;
    const unsigned xw = *reinterpret_cast<const unsigned*>(x + b * DM + 8 * u + 2 * (lane & 3));
    const unsigned ybase = WS_Y + (unsigned)(b * 64 + u) * 4096u;
    v4u g0, g1, g2, g3;
    for (unsigned spin = 0;; ++spin) {
      MEM_FENCE();
      g0 = ld16<AUX_SC1>(rws, lane * 16, ybase);
      g1 = ld16<AUX_SC1>(rws, lane * 16 + 1024, ybase);
      g2 = ld16<AUX_SC1>(rws, lane * 16 + 2048, ybase);
      g3 = ld16<AUX_SC1>(rws, lane * 16 + 3072, ybase);
      const bool ok = g0[1] == tag && g0[3] == tag && g1[1] == tag && g1[3] == tag && g2[1] == tag && g2[3] == tag && g3[1] == tag && g3[3] == tag;
      if (__all(ok) || spin > SPIN_CAP) break;
      __builtin_amdgcn_s_sleep(8);
    }
    float y0 = (__uint_as_float(g0[0]) + __uint_as_float(g1[0])) + (__uint_as_float(g2[0]) + __uint_as_float(g3[0]));
    float y1 = (__uint_as_float(g0[2]) + __uint_as_float(g1[2])) + (__uint_as_float(g2[2]) + __uint_as_float(g3[2]));
    y0 += dpp<0x124>(y0); y1 += dpp<0x124>(y1);
    y0 += dpp<XOR8>(y0); y1 += dpp<XOR8>(y1);
    y0 = swap16_add(y0, y0); y1 = swap16_add(y1, y1);
    y0 = swap32_add(y0, y0); y1 = swap32_add(y1, y1);
    if (lane < 4) *reinterpret_cast<unsigned*>(out + b * DM + 8 * u + 2 * lane) = pk_bf16(bflo(xw) + y0, bfhi(xw) + y1);
    if (u == 0 && lane == 0)
      __builtin_amdgcn_raw_buffer_store_b32(tag, rws, WS_EPOCH + b * 256, 0, AUX_SC1);
  } else {
    const int hw = wave - 4;
    const rsrc_t rx = mk_rsrc(x + b * DM, DM * 2);
    const v4u xr = ld16<0>(rx, lane * 16, 0);
    const unsigned epoch = __builtin_amdgcn_raw_buffer_load_b32(rws, 0, WS_EPOCH + b * 256, AUX_SC1);
    SCHED_FENCE();
    v4u W[32];
    {
      const rsrc_t rq = mk_rsrc(wq + (size_t)(128 * hw) * (NH * DH) + h * DH, 127 * 8192 + 256);
#pragma unroll
      for (int i = 0; i < 32; ++i) W[i] = ld16<0>(rq, r * (32 * 8192) + c * 16, i * 8192);
    }
    SCHED_FENCE();
    unsigned xnp[4];
    {
      float xv[8];
#pragma unroll
      for (int k = 0; k < 4; ++k) { xv[2 * k] = bflo(xr[k]); xv[2 * k + 1] = bfhi(xr[k]); }
      float sm = ((xv[0] + xv[1]) + (xv[2] + xv[3])) + ((xv[4] + xv[5]) + (xv[6] + xv[7]));
      const float mean = wave_sum(sm) * (1.f / DM);
      float sq = 0.f;
#pragma unroll
      for (int k = 0; k < 8; ++k) { const float d = xv[k] - mean; sq = fmaf(d, d, sq); }
      const float var = wave_sum(sq) * (1.f / DM);
      const float mb = rbf(mean);
      const float sd = rbf(sqrtf(rbf(rbf(var) + 1e-5f)));
      const float ri = 1.f / sd;
      float xn[8];
#pragma unroll
      for (int k = 0; k < 8; ++k) {
        const float d = rbf(xv[k] - mb);
        const float q0 = d * ri;
        xn[k] = fmaf(fmaf(-q0, sd, d), ri, q0);
      }
#pragma unroll
      for (int k = 0; k < 4; ++k) xnp[k] = pk_bf16(xn[2 * k], xn[2 * k + 1]);
      *reinterpret_cast<v4u*>(lds + L_XN + hw * 256 + 4 * lane) = v4u{xnp[0], xnp[1], xnp[2], xnp[3]};
    }
    const unsigned tag = epoch + TAG_STEP;
    if (hw == 0 && lane == 0) lds[L_TAG] = tag;
    {
      v4u xp[4];
#pragma unroll
      for (int j = 0; j < 4; ++j) xp[j] = *reinterpret_cast<const v4u*>(lds + L_XN + hw * 256 + 64 * hw + 16 * r + 4 * j);
      float acc[8];
#pragma unroll
      for (int k = 0; k < 8; ++k) acc[k] = 0.f;
#pragma unroll
      for (int j = 0; j < 16; ++j) gemv_pair(acc, W[2 * j], W[2 * j + 1], xp[j >> 2][j & 3]);
      float o0, o1;
      rows_reduce8(acc, o0, o1);
      *reinterpret_cast<f32x2*>(ldsf + L_QP + hw * 128 + 8 * c + 2 * r) = f32x2{o0, o1};
    }
    wg_barrier();
    const int fu = hh * 2 + s;
    const int cb = xg * 7 + (fu < 7 ? fu : 0);
    const unsigned fbytes = fu < 7 ? 1u : 0u;
    {
      const rsrc_t rg = mk_rsrc(wg + 32 * cb + 8 * hw, fbytes * (511 * FF * 2 + 16));
      const rsrc_t ru = mk_rsrc(wu + 32 * cb + 8 * hw, fbytes * (511 * FF * 2 + 16));
      v4u G[8], U[8];
#pragma unroll
      for (int i = 0; i < 8; ++i) G[i] = ld16<0>(rg, lane * (8 * FF * 2), i * (FF * 2));
#pragma unroll
      for (int i = 0; i < 8; ++i) U[i] = ld16<0>(ru, lane * (8 * FF * 2), i * (FF * 2));
      float ag[8], au[8];
#pragma unroll
      for (int k = 0; k < 8; ++k) { ag[k] = 0.f; au[k] = 0.f; }
#pragma unroll
      for (int j = 0; j < 4; ++j) { gemv_pair(ag, G[2 * j], G[2 * j + 1], xnp[j]); gemv_pair(au, U[2 * j], U[2 * j + 1], xnp[j]); }
      float g0, g1, u0, u1;
      rows_reduce8(ag, g0, g1); rows_reduce8(au, u0, u1);
      const float ga = g0 + dpp<XOR8>(g0), gb = g1 + dpp<XOR8>(g1), ua = u0 + dpp<XOR8>(u0), ub = u1 + dpp<XOR8>(u1);
      float gg = (c & 8) ? gb : ga, uu = (c & 8) ? ub : ua;
      gg += dpp<XOR7>(gg); uu += dpp<XOR7>(uu);
      gg += dpp<XOR2>(gg); uu += dpp<XOR2>(uu);
      gg += dpp<XOR1>(gg); uu += dpp<XOR1>(uu);
      gg = rbf(gg); uu = rbf(uu);
      const float sg = rbf(gg / (1.f + __expf(-gg)));
      const float hv = sg * uu;
      if ((c & 7) == 0) reinterpret_cast<__bf16*>(lds + L_HB)[8 * hw + 2 * r + (c >> 3)] = (__bf16)hv;
    }
    v4u WD[8];
    {
      const rsrc_t rd = mk_rsrc(wd + (size_t)(32 * cb) * DM + 128 * hw, fbytes * (31 * 1024 + 256));
#pragma unroll
      for (int i = 0; i < 8; ++i) WD[i] = ld16<0>(rd, r * (8 * 1024) + c * 16, i * 1024);
      const rsrc_t ro = mk_rsrc(wo + (size_t)(h * DH) * DM + 128 * hw, 127 * 1024 + 256);
#pragma unroll
      for (int i = 0; i < 32; ++i) W[i] = ld16<0>(ro, r * (32 * 1024) + c * 16, i * 1024);
    }
#pragma unroll
    for (int j = 0; j < 16; ++j) {
      v4u lo, hi;
#pragma unroll
      for (int k = 0; k < 4; ++k) { lo[k] = perm_lo(W[2 * j][k], W[2 * j + 1][k]); hi[k] = perm_hi(W[2 * j][k], W[2 * j + 1][k]); }
      W[2 * j] = lo; W[2 * j + 1] = hi;
    }
    wg_barrier();
    float y[8];
#pragma unroll
    for (int k = 0; k < 8; ++k) y[k] = 0.f;
    {
      const v4u hp = *reinterpret_cast<const v4u*>(lds + L_HB + 4 * r);
#pragma unroll
      for (int j = 0; j < 4; ++j) gemv_pair(y, WD[2 * j], WD[2 * j + 1], hp[j]);
    }
    float f;
    {
      const unsigned o = lane < 8 ? WS_ML + (unsigned)(bh * 8 + lane) * 16u : 0x7ffffff0u;
      v4u ml;
      for (unsigned spin = 0;; ++spin) {
        MEM_FENCE();
        ml = ld16<AUX_SC1>(rws, o, 0);
        const bool ok = lane >= 8 || (ml[1] == tag && ml[3] == tag);
        if (__all(ok) || spin > SPIN_CAP) break;
        __builtin_amdgcn_s_sleep(32);
      }
      const float m0 = __uint_as_float(__builtin_amdgcn_readlane(ml[0], 0)), m1 = __uint_as_float(__builtin_amdgcn_readlane(ml[0], 4));
      float l0 = 0.f, l1 = 0.f;
#pragma unroll
      for (int w = 0; w < 4; ++w) {
        l0 += __uint_as_float(__builtin_amdgcn_readlane(ml[2], w));
        l1 += __uint_as_float(__builtin_amdgcn_readlane(ml[2], 4 + w));
      }
      const float M = fmaxf(m0, m1);
      const float w0 = __builtin_amdgcn_exp2f((m0 - M) * LOG2E), w1 = __builtin_amdgcn_exp2f((m1 - M) * LOG2E);
      f = (s == 0 ? w0 : w1) / fmaf(w0, l0, w1 * l1);
    }
    wg_barrier();
    {
      float o0 = 0.f, o1 = 0.f;
#pragma unroll
      for (int w = 0; w < 4; ++w) {
        const f32x2 t = *reinterpret_cast<const f32x2*>(ldsf + L_OP + w * 128 + 32 * r + 2 * c);
        o0 += t[0]; o1 += t[1];
      }
      const unsigned opk = pk_bf16(o0 * f, o1 * f);
#define YSTEP(J)                                                                                 \
  {                                                                                              \
    const unsigned ob = dppu<BCAST + (J)>(opk);                                                  \
    _Pragma("unroll") for (int k = 0; k < 4; ++k) {                                              \
      y[2 * k] = dot2(W[2 * (J)][k], ob, y[2 * k]);                                              \
      y[2 * k + 1] = dot2(W[2 * (J) + 1][k], ob, y[2 * k + 1]);                                  \
    }                                                                                            \
  }
      YSTEP(0) YSTEP(1) YSTEP(2) YSTEP(3) YSTEP(4) YSTEP(5) YSTEP(6) YSTEP(7)
      YSTEP(8) YSTEP(9) YSTEP(10) YSTEP(11) YSTEP(12) YSTEP(13) YSTEP(14) YSTEP(15)
#undef YSTEP
      float y0, y1;
      rows_reduce8(y, y0, y1);
      const v4u gr = {__float_as_uint(y0), tag, __float_as_uint(y1), tag};
      const unsigned o = WS_Y + (unsigned)(b * 64 + 16 * hw + c) * 4096u + (unsigned)u * 64u + (unsigned)r * 16u;
      __builtin_amdgcn_raw_buffer_store_b128(gr, rws, o, 0, AUX_SC1);
    }
  }
}

}

extern "C" void kernel_launch(void* const* d_in, const int* in_sizes, int n_in, void* d_out, int out_size, void* d_ws, size_t ws_size,
                              hipStream_t stream) {
  (void)in_sizes; (void)n_in; (void)out_size; (void)ws_size;
  hipLaunchKernelGGL(fused_decode_kernel, dim3(256), dim3(512), 0, stream, (const u16*)d_in[0], (const u16*)d_in[1], (const u16*)d_in[2],
                     (const u16*)d_in[3], (const u16*)d_in[4], (const u16*)d_in[6], (const u16*)d_in[7], (const u16*)d_in[8], (u16*)d_out,
                     (unsigned char*)d_ws);
}
```

```cpp
#include <hip/hip_runtime.h>
#include <hip/hip_bf16.h>
#include <stdint.h>

typedef unsigned short u16;
typedef unsigned v4u __attribute__((ext_vector_type(4)));
typedef unsigned v2u __attribute__((ext_vector_type(2)));
typedef float f32x4 __attribute__((ext_vector_type(4)));
typedef float f32x2 __attribute__((ext_vector_type(2)));
typedef __bf16 bf16x2 __attribute__((ext_vector_type(2)));
typedef __amdgpu_buffer_rsrc_t rsrc_t;
typedef __attribute__((address_space(3))) volatile unsigned lds_vu32;
typedef __attribute__((address_space(3))) volatile v4u lds_vv4u;

namespace {

constexpr int DM = 512, NH = 32, SK = 8192, DH = 128, FF = 1792;
constexpr float SCALE = 0.08838834764831845f;
constexpr float LOG2E = 1.4426950408889634f;
constexpr int AUX_NT = 2;
constexpr int AUX_SC1 = 16;
constexpr unsigned TAG_STEP = 0x9E3779B9u;
constexpr unsigned SPIN_CAP = 1u << 18;
constexpr unsigned OOR = 0x7ffffff0u;

#define DIST 2
#define TAU_NS 2285
#define SA_NS 900
#define SB_NS 100
constexpr int TAU256 = TAU_NS * 256 / 40;
#define QDELAY 450
#define WIN_SHIFT 8
#define WIN_PHASE 64
#define WIN_START 1000
#define ABL_NOW 0

constexpr unsigned WS_EPOCH = 0;
constexpr unsigned WS_ML = 4096;
constexpr unsigned WS_P = WS_ML + 128 * 128;
constexpr unsigned WS_Y = WS_P + 4 * 60 * 8 * 1024;
constexpr unsigned WS_END = WS_Y + 4 * 64 * 64 * 64;

constexpr int L_SC = 0;
constexpr int L_XN = L_SC + 4096;
constexpr int L_QP = L_XN + 1024;
constexpr int L_OP = L_QP + 512;
constexpr int L_GU = L_OP + 512;
constexpr int L_FL = L_GU + 512;
constexpr int L_TAG = L_FL + 16;
constexpr int L_END = L_TAG + 4;

__device__ __forceinline__ rsrc_t mk_rsrc(const void* p, unsigned bytes) {
  return __builtin_amdgcn_make_buffer_rsrc(const_cast<void*>(p), 0, bytes, 0x00020000);
}
template <int AUX> __device__ __forceinline__ v4u ld16(rsrc_t r, unsigned voff, unsigned soff) {
  return __builtin_amdgcn_raw_buffer_load_b128(r, voff, soff, AUX);
}
__device__ __forceinline__ float dot2(unsigned a, unsigned b, float acc) {
  return __builtin_amdgcn_fdot2_f32_bf16(__builtin_bit_cast(bf16x2, a), __builtin_bit_cast(bf16x2, b), acc, false);
}
__device__ __forceinline__ float bflo(unsigned w) { return __uint_as_float(w << 16); }
__device__ __forceinline__ float bfhi(unsigned w) { return __uint_as_float(w & 0xffff0000u); }
__device__ __forceinline__ unsigned pk_bf16(float lo, float hi) {
  bf16x2 v = {(__bf16)lo, (__bf16)hi};
  return __builtin_bit_cast(unsigned, v);
}
__device__ __forceinline__ float rbf(float x) { return (float)(__bf16)x; }
template <int CTRL> __device__ __forceinline__ float dpp(float x) {
  return __builtin_bit_cast(float, __builtin_amdgcn_mov_dpp(__builtin_bit_cast(int, x), CTRL, 0xf, 0xf, true));
}
template <int CTRL> __device__ __forceinline__ unsigned dppu(unsigned x) {
  return (unsigned)__builtin_amdgcn_mov_dpp((int)x, CTRL, 0xf, 0xf, true);
}
constexpr int XOR1 = 0xB1, XOR2 = 0x4E, XOR7 = 0x141, XOR8 = 0x128, ROR4 = 0x124, BCAST = 0x150;
__device__ __forceinline__ float swap32_add(float a, float b) {
  auto t = __builtin_amdgcn_permlane32_swap(__float_as_uint(a), __float_as_uint(b), false, false);
  return __uint_as_float(t[0]) + __uint_as_float(t[1]);
}
__device__ __forceinline__ float swap16_add(float a, float b) {
  auto t = __builtin_amdgcn_permlane16_swap(__float_as_uint(a), __float_as_uint(b), false, false);
  return __uint_as_float(t[0]) + __uint_as_float(t[1]);
}
__device__ __forceinline__ float wave_sum(float x) {
  x += dpp<XOR1>(x); x += dpp<XOR2>(x); x += dpp<XOR7>(x); x += dpp<XOR8>(x);
  x = swap16_add(x, x); x = swap32_add(x, x);
  return x;
}
__device__ __forceinline__ float wave_max(float x) {
  x = fmaxf(x, dpp<XOR1>(x)); x = fmaxf(x, dpp<XOR2>(x)); x = fmaxf(x, dpp<XOR7>(x)); x = fmaxf(x, dpp<XOR8>(x));
  auto s = __builtin_amdgcn_permlane16_swap(__float_as_uint(x), __float_as_uint(x), false, false);
  x = fmaxf(__uint_as_float(s[0]), __uint_as_float(s[1]));
  auto t = __builtin_amdgcn_permlane32_swap(__float_as_uint(x), __float_as_uint(x), false, false);
  return fmaxf(__uint_as_float(t[0]), __uint_as_float(t[1]));
}
__device__ __forceinline__ void rows_reduce8(const float (&a)[8], float& o0, float& o1) {
  const float t0 = swap32_add(a[0], a[4]), t1 = swap32_add(a[1], a[5]), t2 = swap32_add(a[2], a[6]), t3 = swap32_add(a[3], a[7]);
  o0 = swap16_add(t0, t2); o1 = swap16_add(t1, t3);
}
__device__ __forceinline__ void wg_barrier() { asm volatile("s_waitcnt lgkmcnt(0)\n\ts_barrier" ::: "memory"); }
#define SCHED_FENCE() __builtin_amdgcn_sched_barrier(0)
#define MEM_FENCE() asm volatile("" ::: "memory")
#define PIN4(a) asm volatile("" : "+v"((a)[0]), "+v"((a)[1]), "+v"((a)[2]), "+v"((a)[3]) :: "memory")
#define PIN8(a) asm volatile("" : "+v"((a)[0]), "+v"((a)[1]), "+v"((a)[2]), "+v"((a)[3]), "+v"((a)[4]), "+v"((a)[5]), "+v"((a)[6]), "+v"((a)[7]) :: "memory")
__device__ __forceinline__ unsigned perm_lo(unsigned a, unsigned b) { return __builtin_amdgcn_perm(b, a, 0x05040100u); }
__device__ __forceinline__ unsigned perm_hi(unsigned a, unsigned b) { return __builtin_amdgcn_perm(b, a, 0x07060302u); }
__device__ __forceinline__ void gemv_pair(float (&acc)[8], const v4u& ra, const v4u& rb, unsigned xp) {
#pragma unroll
  for (int k = 0; k < 4; ++k) {
    acc[2 * k] = dot2(perm_lo(ra[k], rb[k]), xp, acc[2 * k]);
    acc[2 * k + 1] = dot2(perm_hi(ra[k], rb[k]), xp, acc[2 * k + 1]);
  }
}
__device__ __forceinline__ void lds_signal(unsigned* fl, int lane) {
  asm volatile("s_waitcnt lgkmcnt(0)" ::: "memory");
  if (lane == 0) *(lds_vu32*)fl = 1u;
}
__device__ __forceinline__ void lds_wait4(unsigned* fl4) {
#pragma unroll 1
  for (unsigned spin = 0; spin < (SPIN_CAP << 4); ++spin) {
    const v4u f = *(lds_vv4u*)fl4;
    if ((f[0] & f[1] & f[2] & f[3]) != 0u) break;
  }
  MEM_FENCE();
}
__device__ __forceinline__ unsigned now32() { return (unsigned)__builtin_amdgcn_s_memrealtime(); }
template <int SLEEP> __device__ __forceinline__ void wait_until(unsigned t) {
#pragma unroll 1
  for (int sp = 0; sp < 4096; ++sp) {
    if ((int)(now32() - t) >= 0) break;
    __builtin_amdgcn_s_sleep(SLEEP);
  }
  MEM_FENCE();
  SCHED_FENCE();
}
__device__ __forceinline__ void proj_part(const v4u* W, const v2u (&xp)[4], float* dst) {
  unsigned wp[16];
#pragma unroll
  for (int k = 0; k < 4; ++k) {
    wp[2 * k] = perm_lo(W[0][k], W[1][k]); wp[2 * k + 1] = perm_hi(W[0][k], W[1][k]);
    wp[8 + 2 * k] = perm_lo(W[2][k], W[3][k]); wp[8 + 2 * k + 1] = perm_hi(W[2][k], W[3][k]);
  }
  float t[8];
#pragma unroll
  for (int k = 0; k < 8; ++k) {
    const float a0 = dot2(wp[8 + k], xp[0][1], dot2(wp[k], xp[0][0], 0.f)), a1 = dot2(wp[8 + k], xp[1][1], dot2(wp[k], xp[1][0], 0.f));
    const float a2 = dot2(wp[8 + k], xp[2][1], dot2(wp[k], xp[2][0], 0.f)), a3 = dot2(wp[8 + k], xp[3][1], dot2(wp[k], xp[3][0], 0.f));
    t[k] = swap16_add(swap32_add(a0, a2), swap32_add(a1, a3));
  }
  *reinterpret_cast<f32x4*>(dst) = f32x4{t[0], t[1], t[2], t[3]};
  *reinterpret_cast<f32x4*>(dst + 4) = f32x4{t[4], t[5], t[6], t[7]};
}

__device__ __forceinline__ void ld_quarter(v4u* R, rsrc_t rk, rsrc_t rv, unsigned voff, int n, int rot, unsigned tb) {
  if (TAU256 > 0) {
    const unsigned due = tb + (unsigned)(SA_NS / 10) + (unsigned)((n * TAU256) >> 8) + (unsigned)((n < 12 ? n : 12) * (SB_NS / 10));
#pragma unroll 1
    for (int sp = 0; sp < 512; ++sp) {
      if ((int)(now32() - due) >= 0 || n >= 128) break;
      __builtin_amdgcn_s_sleep(1);
    }
    SCHED_FENCE();
  }
  const rsrc_t rn = n < 64 ? rk : rv;
  const unsigned off = n < 128 ? ((unsigned)(((n >> 1) + rot) & 31) << 15) + ((unsigned)(n & 1) << 12) : (1u << 20);
#pragma unroll
  for (int i = 0; i < 4; ++i) R[i] = ld16<AUX_NT>(rn, voff + i * 1024, off);
  SCHED_FENCE();
}
#define WAIT_Q() asm volatile("s_waitcnt vmcnt(4)" ::: "memory")
#define WAIT_PRO() asm volatile("s_waitcnt vmcnt(4)" ::: "memory")

template <bool REFILL, int SKIP = 0>
__device__ __forceinline__ float k_group(v4u* R, const v4u& q, int c, rsrc_t rk, rsrc_t rv, unsigned voff, int n0, int rot, unsigned tb) {
  float x[16];
#pragma unroll
  for (int k = 0; k < 4; ++k) {
    if (REFILL && k >= SKIP) {
      WAIT_Q();
      ld_quarter(R + 4 * ((k + DIST) & 3), rk, rv, voff, n0 + k + DIST, rot, tb);
    }
#pragma unroll
    for (int i = 4 * k; i < 4 * k + 4; ++i) {
      float d = dot2(R[i][0], q[0], 0.f); d = dot2(R[i][1], q[1], d); d = dot2(R[i][2], q[2], d); x[i] = dot2(R[i][3], q[3], d);
    }
    PIN4(x + 4 * k);
    SCHED_FENCE();
  }
#define TR_STEP(HALF, CTRL, BIT)                                                                 \
  _Pragma("unroll") for (int hh_ = 0; hh_ < (HALF); ++hh_) {                                     \
    const float lo_ = x[hh_], hi_ = x[hh_ + (HALF)];                                             \
    const float t1_ = lo_ + dpp<CTRL>(lo_), t2_ = hi_ + dpp<CTRL>(hi_);                          \
    x[hh_] = (c & (BIT)) ? t2_ : t1_;                                                            \
  }
  TR_STEP(8, XOR8, 8) TR_STEP(4, XOR7, 4) TR_STEP(2, XOR2, 2) TR_STEP(1, XOR1, 1)
#undef TR_STEP
  return rbf(rbf(x[0]) * SCALE);
}

template <int I> __device__ __forceinline__ void pv_one(float (&acc)[8], const v4u& V, float p) {
  const float pb = dpp<BCAST + I>(p);
#pragma unroll
  for (int k = 0; k < 4; ++k) {
    acc[2 * k] = fmaf(pb, bflo(V[k]), acc[2 * k]);
    acc[2 * k + 1] = fmaf(pb, bfhi(V[k]), acc[2 * k + 1]);
  }
}

#define STAMP(i) do { } while (0)

__global__ void __launch_bounds__(512, 2)
fused_decode_kernel(const u16* __restrict__ x, const u16* __restrict__ kc, const u16* __restrict__ vc, const u16* __restrict__ wq,
                    const u16* __restrict__ wo, const u16* __restrict__ wg, const u16* __restrict__ wu, const u16* __restrict__ wd,
                    u16* __restrict__ out, unsigned char* ws) {
  __shared__ __attribute__((aligned(16))) unsigned lds[L_END];
  float* ldsf = reinterpret_cast<float*>(lds);
  const int tid = threadIdx.x, lane = tid & 63;
  const int wave = __builtin_amdgcn_readfirstlane(tid >> 6) ^ 4;
  const int r = lane >> 4, c = lane & 15;
  const int bid = blockIdx.x, xg = bid & 7, idx = bid >> 3;
  const int hh = idx >> 3, h = xg + 8 * hh, b = (idx >> 1) & 3, s = idx & 1;
  const int bh = b * NH + h, u = h * 2 + s;
  const rsrc_t rws = mk_rsrc(ws, WS_END);
  const unsigned tb = now32();
  STAMP(0);

  if (wave < 4) {
    const size_t kvoff = ((size_t)bh * SK + (size_t)s * (SK / 2)) * DH;
    const rsrc_t rk = mk_rsrc(kc + kvoff, 1u << 20), rv = mk_rsrc(vc + kvoff, 1u << 20);
    const unsigned voff = lane * 16 + wave * 8192;
    const int rot = idx;
    v4u P[32], R[16];
    wg_barrier();
    ld_quarter(P, rk, rv, voff, 0, rot, tb);
    ld_quarter(P + 4, rk, rv, voff, 1, rot, tb);
#pragma unroll
    for (int k = 2; k < 8; ++k) { WAIT_PRO(); ld_quarter(P + 4 * k, rk, rv, voff, k, rot, tb); }
#pragma unroll
    for (int k = 0; k < 4; ++k) { WAIT_PRO(); ld_quarter(R + 4 * k, rk, rv, voff, 8 + k, rot, tb); }
    wg_barrier();
    STAMP(1);
    v4u q;
    {
      float qs[8];
#pragma unroll
      for (int k = 0; k < 8; ++k) qs[k] = 0.f;
#pragma unroll
      for (int w = 0; w < 4; ++w) {
        const f32x4 a0 = *reinterpret_cast<const f32x4*>(ldsf + L_QP + w * 128 + 8 * c);
        const f32x4 a1 = *reinterpret_cast<const f32x4*>(ldsf + L_QP + w * 128 + 8 * c + 4);
#pragma unroll
        for (int k = 0; k < 4; ++k) { qs[k] += a0[k]; qs[4 + k] += a1[k]; }
      }
#pragma unroll
      for (int k = 0; k < 4; ++k) q[k] = pk_bf16(qs[2 * k], qs[2 * k + 1]);
    }
    const unsigned tag = lds[L_TAG];
    float* sc = ldsf + L_SC + wave * 1024 + lane;
    float mx = k_group<true, 4 - DIST>(R, q, c, rk, rv, voff, 8, rot, tb);
    sc[2 * 64] = mx;
#pragma unroll
    for (int g = 0; g < 2; ++g) {
      const float sv = k_group<false>(P + 16 * g, q, c, rk, rv, voff, 0, rot, tb);
      sc[g * 64] = sv;
      mx = fmaxf(mx, sv);
    }
#pragma unroll 1
    for (int g = 3; g < 16; ++g) {
      const float sv = k_group<true>(R, q, c, rk, rv, voff, 4 * g, rot, tb);
      sc[g * 64] = sv;
      mx = fmaxf(mx, sv);
    }
    STAMP(2);
    mx = wave_max(mx);
    float l = 0.f;
#pragma unroll
    for (int g = 0; g < 16; ++g) {
      const float p = __builtin_amdgcn_exp2f((sc[g * 64] - mx) * LOG2E);
      sc[g * 64] = p;
      l += p;
    }
    l = wave_sum(l);
    __builtin_amdgcn_raw_buffer_store_b128(v4u{__float_as_uint(mx), tag, __float_as_uint(l), tag}, rws,
                                           lane == 0 ? WS_ML + (unsigned)((bh * 2 + s) * 4 + wave) * 16u : OOR, 0, AUX_SC1);
    STAMP(3);
    float acc[8];
#pragma unroll
    for (int k = 0; k < 8; ++k) acc[k] = 0.f;
#pragma unroll 1
    for (int g = 0; g < 16; ++g) {
      const float p = sc[g * 64];
      if (g == 10 && lane == 0) *(lds_vu32*)(lds + L_FL + 12 + wave) = 1u;
      const int n0 = 64 + 4 * g + DIST;
#define PV_Q(k)                                                                                  \
      WAIT_Q();                                                                                  \
      ld_quarter(R + 4 * (((k) + DIST) & 3), rv, rv, voff, n0 + (k), rot, tb);                   \
      pv_one<4 * (k)>(acc, R[4 * (k)], p); pv_one<4 * (k) + 1>(acc, R[4 * (k) + 1], p);          \
      pv_one<4 * (k) + 2>(acc, R[4 * (k) + 2], p); pv_one<4 * (k) + 3>(acc, R[4 * (k) + 3], p);  \
      PIN8(acc);                                                                                 \
      SCHED_FENCE();
      PV_Q(0) PV_Q(1) PV_Q(2) PV_Q(3)
#undef PV_Q
    }
    {
      float o0, o1;
      rows_reduce8(acc, o0, o1);
      *reinterpret_cast<f32x2*>(ldsf + L_OP + wave * 128 + 8 * c + 2 * r) = f32x2{o0, o1};
    }
    STAMP(4);
    wg_barrier();
    STAMP(5);
    if (wave != 0) return;
    const unsigned xw = *reinterpret_cast<const unsigned*>(x + b * DM + 8 * u + 2 * (lane & 3));
    const unsigned ybase = WS_Y + (unsigned)(b * 64 + u) * 4096u;
    v4u g0, g1, g2, g3;
#pragma unroll 1
    for (unsigned spin = 0;; ++spin) {
      MEM_FENCE();
      g0 = ld16<AUX_SC1>(rws, lane * 16, ybase);
      g1 = ld16<AUX_SC1>(rws, lane * 16 + 1024, ybase);
      g2 = ld16<AUX_SC1>(rws, lane * 16 + 2048, ybase);
      g3 = ld16<AUX_SC1>(rws, lane * 16 + 3072, ybase);
      const bool ok = g0[1] == tag && g0[3] == tag && g1[1] == tag && g1[3] == tag && g2[1] == tag && g2[3] == tag && g3[1] == tag && g3[3] == tag;
      if (__all(ok) || spin > SPIN_CAP) break;
      __builtin_amdgcn_s_sleep(8);
    }
    float y0 = (__uint_as_float(g0[0]) + __uint_as_float(g1[0])) + (__uint_as_float(g2[0]) + __uint_as_float(g3[0]));
    float y1 = (__uint_as_float(g0[2]) + __uint_as_float(g1[2])) + (__uint_as_float(g2[2]) + __uint_as_float(g3[2]));
    y0 += dpp<ROR4>(y0); y1 += dpp<ROR4>(y1);
    y0 += dpp<XOR8>(y0); y1 += dpp<XOR8>(y1);
    y0 = swap16_add(y0, y0); y1 = swap16_add(y1, y1);
    y0 = swap32_add(y0, y0); y1 = swap32_add(y1, y1);
    if (lane < 4) *reinterpret_cast<unsigned*>(out + b * DM + 8 * u + 2 * lane) = pk_bf16(bflo(xw) + y0, bfhi(xw) + y1);
    STAMP(6);
    if (u == 0 && b == 0 && lane == 0) __builtin_amdgcn_raw_buffer_store_b32(tag, rws, WS_EPOCH, 0, AUX_SC1);
  } else {
    const int hw = wave - 4;
    const v4u xr = ld16<0>(mk_rsrc(x + hw * DM, DM * 2), lane * 16, 0);
    const unsigned epoch = __builtin_amdgcn_raw_buffer_load_b32(rws, 0, WS_EPOCH, AUX_SC1);
    SCHED_FENCE();
    const int cq = xg * 32 + idx, pb = cq >> 3, kq = cq & 7;
    v4u W[32];
    {
      const rsrc_t rq = mk_rsrc(wq + (size_t)(64 * kq + 16 * hw) * (NH * DH) + pb * DH, 15 * 8192 + 256);
#pragma unroll
      for (int i = 0; i < 4; ++i) W[i] = ld16<AUX_NT>(rq, r * (4 * 8192) + c * 16, i * 8192);
      const u16* wf = pb < 14 ? wg + pb * 128 : wu + (pb < 28 ? pb - 14 : 0) * 128;
      const rsrc_t rf = mk_rsrc(wf + (size_t)(64 * kq + 16 * hw) * FF, (pb < 28 && !ABL_NOW) ? 15 * FF * 2 + 256 : 0);
#pragma unroll
      for (int i = 0; i < 4; ++i) W[4 + i] = ld16<AUX_NT>(rf, r * (4 * FF * 2) + c * 16, i * (FF * 2));
    }
    SCHED_FENCE();
    if (hw == 0 && lane < 16) lds[L_FL + lane] = 0u;
    wg_barrier();
    {
      float xv[8];
#pragma unroll
      for (int k = 0; k < 4; ++k) { xv[2 * k] = bflo(xr[k]); xv[2 * k + 1] = bfhi(xr[k]); }
      const float sm = ((xv[0] + xv[1]) + (xv[2] + xv[3])) + ((xv[4] + xv[5]) + (xv[6] + xv[7]));
      const float mean = wave_sum(sm) * (1.f / DM);
      float sq = 0.f;
#pragma unroll
      for (int k = 0; k < 8; ++k) { const float d = xv[k] - mean; sq = fmaf(d, d, sq); }
      const float var = wave_sum(sq) * (1.f / DM);
      const float mb = rbf(mean);
      const float sd = rbf(sqrtf(rbf(rbf(var) + 1e-5f)));
      const float ri = 1.f / sd;
      float xn[8];
#pragma unroll
      for (int k = 0; k < 8; ++k) {
        const float d = rbf(xv[k] - mb);
        const float q0 = d * ri;
        xn[k] = fmaf(fmaf(-q0, sd, d), ri, q0);
      }
      *reinterpret_cast<v4u*>(lds + L_XN + hw * 256 + 4 * lane) =
          v4u{pk_bf16(xn[0], xn[1]), pk_bf16(xn[2], xn[3]), pk_bf16(xn[4], xn[5]), pk_bf16(xn[6], xn[7])};
    }
    const unsigned tag = epoch + TAG_STEP;
    if (hw == 0 && lane == 0) lds[L_TAG] = tag;
    lds_signal(lds + L_FL + hw, lane);
    STAMP(1);
    lds_wait4(lds + L_FL);
    {
      v2u xp[4];
#pragma unroll
      for (int bb = 0; bb < 4; ++bb) xp[bb] = *reinterpret_cast<const v2u*>(lds + L_XN + bb * 256 + 32 * kq + 8 * hw + 2 * r);
      proj_part(W, xp, ldsf + L_SC + (hw * 4 + r) * 128 + 8 * c);
      proj_part(W + 4, xp, ldsf + L_SC + 2048 + (hw * 4 + r) * 128 + 8 * c);
      lds_signal(lds + L_FL + 4 + hw, lane);
      lds_wait4(lds + L_FL + 4);
      float q0 = 0.f, q1 = 0.f, f0 = 0.f, f1 = 0.f;
#pragma unroll
      for (int w = 0; w < 4; ++w) {
        const f32x2 tq = *reinterpret_cast<const f32x2*>(ldsf + L_SC + (w * 4 + hw) * 128 + 2 * lane);
        const f32x2 tf = *reinterpret_cast<const f32x2*>(ldsf + L_SC + 2048 + (w * 4 + hw) * 128 + 2 * lane);
        q0 += tq[0]; q1 += tq[1]; f0 += tf[0]; f1 += tf[1];
      }
      __builtin_amdgcn_raw_buffer_store_b128(v4u{__float_as_uint(q0), tag, __float_as_uint(q1), tag}, rws, lane * 16,
                                             WS_P + (unsigned)((hw * 60 + pb) * 8 + kq) * 1024u, AUX_SC1);
      __builtin_amdgcn_raw_buffer_store_b128(v4u{__float_as_uint(f0), tag, __float_as_uint(f1), tag}, rws, pb < 28 ? lane * 16 : OOR,
                                             WS_P + (unsigned)((hw * 60 + 32 + (pb < 28 ? pb : 0)) * 8 + kq) * 1024u, AUX_SC1);
    }
    {
      const unsigned qb = WS_P + (unsigned)((b * 60 + h) * 8 + 2 * hw) * 1024u;
      wait_until<4>(tb + QDELAY);
      v4u pa, pc;
#pragma unroll 1
      for (unsigned spin = 0;; ++spin) {
        MEM_FENCE();
        pa = ld16<AUX_SC1>(rws, lane * 16, qb);
        pc = ld16<AUX_SC1>(rws, lane * 16 + 1024, qb);
        if (__all(pa[1] == tag && pa[3] == tag && pc[1] == tag && pc[3] == tag) || spin > SPIN_CAP) break;
        __builtin_amdgcn_s_sleep(8);
      }
      *reinterpret_cast<f32x2*>(ldsf + L_QP + hw * 128 + 2 * lane) =
          f32x2{__uint_as_float(pa[0]) + __uint_as_float(pc[0]), __uint_as_float(pa[2]) + __uint_as_float(pc[2])};
    }
    STAMP(2);
    wg_barrier();
    unsigned hp;
    {
      const int seg = lane / 14, piece = lane - seg * 14;
      const int col = 28 * u + 2 * piece, kk = 2 * hw + (seg & 1), mat = seg >> 1;
      const unsigned o = lane < 56 ? WS_P + (unsigned)(((b * 60 + 32 + 14 * mat + (col >> 7)) * 8 + kk) * 128 + (col & 127)) * 8u : OOR;
      v4u gu;
#pragma unroll 1
      for (unsigned spin = 0;; ++spin) {
        MEM_FENCE();
        gu = ld16<AUX_SC1>(rws, o, 0);
        if (__all(lane >= 56 || (gu[1] == tag && gu[3] == tag)) || spin > SPIN_CAP || ABL_NOW) break;
        __builtin_amdgcn_s_sleep(64);
      }
      if (lane < 56) *reinterpret_cast<v2u*>(lds + L_GU + (kk * 2 + mat) * 32 + 2 * piece) = v2u{gu[0], gu[2]};
      lds_signal(lds + L_FL + 8 + hw, lane);
      lds_wait4(lds + L_FL + 8);
      const int j = 8 * r + (c & 7);
      float gg = 0.f, uu = 0.f;
#pragma unroll
      for (int k8 = 0; k8 < 8; ++k8) { gg += ldsf[L_GU + k8 * 64 + j]; uu += ldsf[L_GU + k8 * 64 + 32 + j]; }
      gg = rbf(gg); uu = rbf(uu);
      const float sg = rbf(gg / (1.f + __expf(-gg)));
      const float hv = j < 28 ? sg * uu : 0.f;
      hp = pk_bf16(hv, dpp<XOR1>(hv));
      asm volatile("" : "+v"(hp));
    }
    const unsigned wphase = (unsigned)hh * WIN_PHASE;
    const unsigned cyc0 = ((((tb + WIN_START + wphase) >> (WIN_SHIFT + 1)) + 1u) << (WIN_SHIFT + 1)) - wphase;
    float y[8];
#pragma unroll
    for (int k = 0; k < 8; ++k) y[k] = 0.f;
    {
      const rsrc_t rd = mk_rsrc(wd + (size_t)(28 * u) * DM + 128 * hw, ABL_NOW ? 0 : 27 * 1024 + 256);
#define DSTEP(J)                                                                                 \
      {                                                                                          \
        wait_until<2>(cyc0 + ((unsigned)(J) << WIN_SHIFT));                                      \
        const v4u d0 = ld16<AUX_NT>(rd, r * (8 * 1024) + c * 16, (2 * (J)) * 1024);              \
        const v4u d1 = ld16<AUX_NT>(rd, r * (8 * 1024) + c * 16, (2 * (J) + 1) * 1024);          \
        gemv_pair(y, d0, d1, dppu<BCAST + 2 * (J)>(hp));                                         \
        PIN8(y);                                                                                 \
      }
      DSTEP(0) DSTEP(1) DSTEP(2) DSTEP(3)
#undef DSTEP
      const rsrc_t ro = mk_rsrc(wo + (size_t)(h * DH) * DM + 128 * hw, ABL_NOW ? 0 : 127 * 1024 + 256);
#pragma unroll
      for (int j = 0; j < 16; ++j) {
        wait_until<2>(cyc0 + ((unsigned)(4 + j) << WIN_SHIFT));
        const v4u w0 = ld16<AUX_NT>(ro, r * (32 * 1024) + c * 16, (2 * j) * 1024);
        const v4u w1 = ld16<AUX_NT>(ro, r * (32 * 1024) + c * 16, (2 * j + 1) * 1024);
#pragma unroll
        for (int k = 0; k < 4; ++k) { W[2 * j][k] = perm_lo(w0[k], w1[k]); W[2 * j + 1][k] = perm_hi(w0[k], w1[k]); }
        asm volatile("" : "+v"(W[2 * j]), "+v"(W[2 * j + 1]) :: "memory");
      }
    }
    STAMP(3);
    float f[4];
    {
#pragma unroll 1
      for (unsigned spin = 0; spin < SPIN_CAP; ++spin) {
        if (*(lds_vu32*)(lds + L_FL + 12 + hw) != 0u) break;
        __builtin_amdgcn_s_sleep(32);
      }
      const unsigned o = lane < 8 ? WS_ML + (unsigned)(bh * 8 + lane) * 16u : OOR;
      v4u ml;
#pragma unroll 1
      for (unsigned spin = 0;; ++spin) {
        MEM_FENCE();
        ml = ld16<AUX_SC1>(rws, o, 0);
        if (__all(lane >= 8 || (ml[1] == tag && ml[3] == tag)) || spin > SPIN_CAP) break;
        __builtin_amdgcn_s_sleep(64);
      }
      const float mw = lane < 8 ? __uint_as_float(ml[0]) : -INFINITY;
      float M = fmaxf(mw, dpp<XOR1>(mw)); M = fmaxf(M, dpp<XOR2>(M)); M = fmaxf(M, dpp<XOR7>(M));
      const float wt = lane < 8 ? __builtin_amdgcn_exp2f((mw - M) * LOG2E) : 0.f;
      float L = wt * (lane < 8 ? __uint_as_float(ml[2]) : 0.f);
      L += dpp<XOR1>(L); L += dpp<XOR2>(L); L += dpp<XOR7>(L);
      const float fw = wt / L;
#pragma unroll
      for (int w = 0; w < 4; ++w) f[w] = __uint_as_float(__builtin_amdgcn_readlane(__float_as_uint(fw), 4 * s + w));
      PIN8(y);
    }
    STAMP(4);
    wg_barrier();
    STAMP(5);
    {
      float o0 = 0.f, o1 = 0.f;
#pragma unroll
      for (int w = 0; w < 4; ++w) {
        const f32x2 t = *reinterpret_cast<const f32x2*>(ldsf + L_OP + w * 128 + 32 * r + 2 * c);
        o0 = fmaf(f[w], t[0], o0); o1 = fmaf(f[w], t[1], o1);
      }
      const unsigned opk = pk_bf16(o0, o1);
#define YSTEP(J)                                                                                 \
  {                                                                                              \
    const unsigned ob = dppu<BCAST + (J)>(opk);                                                  \
    _Pragma("unroll") for (int k = 0; k < 4; ++k) {                                              \
      y[2 * k] = dot2(W[2 * (J)][k], ob, y[2 * k]);                                              \
      y[2 * k + 1] = dot2(W[2 * (J) + 1][k], ob, y[2 * k + 1]);                                  \
    }                                                                                            \
  }
      YSTEP(0) YSTEP(1) YSTEP(2) YSTEP(3) YSTEP(4) YSTEP(5) YSTEP(6) YSTEP(7)
      YSTEP(8) YSTEP(9) YSTEP(10) YSTEP(11) YSTEP(12) YSTEP(13) YSTEP(14) YSTEP(15)
#undef YSTEP
      float y0, y1;
      rows_reduce8(y, y0, y1);
      const unsigned o = WS_Y + (unsigned)(b * 64 + 16 * hw + c) * 4096u + (unsigned)u * 64u + (unsigned)r * 16u;
      __builtin_amdgcn_raw_buffer_store_b128(v4u{__float_as_uint(y0), tag, __float_as_uint(y1), tag}, rws, o, 0, AUX_SC1);
      STAMP(6);
    }
  }
}

}

extern "C" void kernel_launch(void* const* d_in, const int* in_sizes, int n_in, void* d_out, int out_size, void* d_ws, size_t ws_size,
                              hipStream_t stream) {
  (void)in_sizes; (void)n_in; (void)out_size; (void)ws_size;
  hipLaunchKernelGGL(fused_decode_kernel, dim3(256), dim3(512), 0, stream, (const u16*)d_in[0], (const u16*)d_in[1], (const u16*)d_in[2],
                     (const u16*)d_in[3], (const u16*)d_in[4], (const u16*)d_in[6], (const u16*)d_in[7], (const u16*)d_in[8], (u16*)d_out,
                     (unsigned char*)d_ws);
}
```

```cpp
#include <hip/hip_runtime.h>
#include <hip/hip_bf16.h>
#include <stdint.h>

typedef unsigned short u16;
typedef unsigned v4u __attribute__((ext_vector_type(4)));
typedef unsigned v2u __attribute__((ext_vector_type(2)));
typedef float f32x4 __attribute__((ext_vector_type(4)));
typedef float f32x2 __attribute__((ext_vector_type(2)));
typedef __bf16 bf16x2 __attribute__((ext_vector_type(2)));
typedef __amdgpu_buffer_rsrc_t rsrc_t;
typedef __attribute__((address_space(3))) volatile unsigned lds_vu32;
typedef __attribute__((address_space(3))) volatile v4u lds_vv4u;

namespace {

constexpr int DM = 512, NH = 32, SK = 8192, DH = 128, FF = 1792;
constexpr float SCALE = 0.08838834764831845f;
constexpr float LOG2E = 1.4426950408889634f;
constexpr int AUX_NT = 2;
constexpr int AUX_SC1 = 16;
constexpr unsigned TAG_STEP = 0x9E3779B9u;
constexpr unsigned SPIN_CAP = 1u << 18;
constexpr unsigned OOR = 0x7ffffff0u;

#define DIST 2
#define TAU_NS 2285
#define SA_NS 900
#define SB_NS 100
constexpr int TAU256 = TAU_NS * 256 / 40;
#define QDELAY 450
#define WIN_SHIFT 8
#define WIN_PHASE 64
#define WIN_START 1000
#define AUX_K 2
#define AUX_V 2
#define ABL_NOW 0

constexpr unsigned WS_EPOCH = 0;
constexpr unsigned WS_ML = 4096;
constexpr unsigned WS_P = WS_ML + 128 * 128;
constexpr unsigned WS_Y = WS_P + 4 * 60 * 8 * 1024;
constexpr unsigned WS_END = WS_Y + 4 * 64 * 64 * 64;

constexpr int L_SC = 0;
constexpr int L_XN = L_SC + 4096;
constexpr int L_QP = L_XN + 1024;
constexpr int L_OP = L_QP + 512;
constexpr int L_GU = L_OP + 512;
constexpr int L_FL = L_GU + 512;
constexpr int L_TAG = L_FL + 16;
constexpr int L_END = L_TAG + 4;

__device__ __forceinline__ rsrc_t mk_rsrc(const void* p, unsigned bytes) {
  return __builtin_amdgcn_make_buffer_rsrc(const_cast<void*>(p), 0, bytes, 0x00020000);
}
template <int AUX> __device__ __forceinline__ v4u ld16(rsrc_t r, unsigned voff, unsigned soff) {
  return __builtin_amdgcn_raw_buffer_load_b128(r, voff, soff, AUX);
}
__device__ __forceinline__ float dot2(unsigned a, unsigned b, float acc) {
  return __builtin_amdgcn_fdot2_f32_bf16(__builtin_bit_cast(bf16x2, a), __builtin_bit_cast(bf16x2, b), acc, false);
}
__device__ __forceinline__ float bflo(unsigned w) { return __uint_as_float(w << 16); }
__device__ __forceinline__ float bfhi(unsigned w) { return __uint_as_float(w & 0xffff0000u); }
__device__ __forceinline__ unsigned pk_bf16(float lo, float hi) {
  bf16x2 v = {(__bf16)lo, (__bf16)hi};
  return __builtin_bit_cast(unsigned, v);
}
__device__ __forceinline__ float rbf(float x) { return (float)(__bf16)x; }
template <int CTRL> __device__ __forceinline__ float dpp(float x) {
  return __builtin_bit_cast(float, __builtin_amdgcn_mov_dpp(__builtin_bit_cast(int, x), CTRL, 0xf, 0xf, true));
}
template <int CTRL> __device__ __forceinline__ unsigned dppu(unsigned x) {
  return (unsigned)__builtin_amdgcn_mov_dpp((int)x, CTRL, 0xf, 0xf, true);
}
constexpr int XOR1 = 0xB1, XOR2 = 0x4E, XOR7 = 0x141, XOR8 = 0x128, ROR4 = 0x124, BCAST = 0x150;
__device__ __forceinline__ float swap32_add(float a, float b) {
  auto t = __builtin_amdgcn_permlane32_swap(__float_as_uint(a), __float_as_uint(b), false, false);
  return __uint_as_float(t[0]) + __uint_as_float(t[1]);
}
__device__ __forceinline__ float swap16_add(float a, float b) {
  auto t = __builtin_amdgcn_permlane16_swap(__float_as_uint(a), __float_as_uint(b), false, false);
  return __uint_as_float(t[0]) + __uint_as_float(t[1]);
}
__device__ __forceinline__ float wave_sum(float x) {
  x += dpp<XOR1>(x); x += dpp<XOR2>(x); x += dpp<XOR7>(x); x += dpp<XOR8>(x);
  x = swap16_add(x, x); x = swap32_add(x, x);
  return x;
}
__device__ __forceinline__ float wave_max(float x) {
  x = fmaxf(x, dpp<XOR1>(x)); x = fmaxf(x, dpp<XOR2>(x)); x = fmaxf(x, dpp<XOR7>(x)); x = fmaxf(x, dpp<XOR8>(x));
  auto s = __builtin_amdgcn_permlane16_swap(__float_as_uint(x), __float_as_uint(x), false, false);
  x = fmaxf(__uint_as_float(s[0]), __uint_as_float(s[1]));
  auto t = __builtin_amdgcn_permlane32_swap(__float_as_uint(x), __float_as_uint(x), false, false);
  return fmaxf(__uint_as_float(t[0]), __uint_as_float(t[1]));
}
__device__ __forceinline__ void rows_reduce8(const float (&a)[8], float& o0, float& o1) {
  const float t0 = swap32_add(a[0], a[4]), t1 = swap32_add(a[1], a[5]), t2 = swap32_add(a[2], a[6]), t3 = swap32_add(a[3], a[7]);
  o0 = swap16_add(t0, t2); o1 = swap16_add(t1, t3);
}
__device__ __forceinline__ void wg_barrier() { asm volatile("s_waitcnt lgkmcnt(0)\n\ts_barrier" ::: "memory"); }
#define SCHED_FENCE() __builtin_amdgcn_sched_barrier(0)
#define MEM_FENCE() asm volatile("" ::: "memory")
#define PIN4(a) asm volatile("" : "+v"((a)[0]), "+v"((a)[1]), "+v"((a)[2]), "+v"((a)[3]) :: "memory")
#define PIN8(a) asm volatile("" : "+v"((a)[0]), "+v"((a)[1]), "+v"((a)[2]), "+v"((a)[3]), "+v"((a)[4]), "+v"((a)[5]), "+v"((a)[6]), "+v"((a)[7]) :: "memory")
__device__ __forceinline__ unsigned perm_lo(unsigned a, unsigned b) { return __builtin_amdgcn_perm(b, a, 0x05040100u); }
__device__ __forceinline__ unsigned perm_hi(unsigned a, unsigned b) { return __builtin_amdgcn_perm(b, a, 0x07060302u); }
__device__ __forceinline__ void gemv_pair(float (&acc)[8], const v4u& ra, const v4u& rb, unsigned xp) {
#pragma unroll
  for (int k = 0; k < 4; ++k) {
    acc[2 * k] = dot2(perm_lo(ra[k], rb[k]), xp, acc[2 * k]);
    acc[2 * k + 1] = dot2(perm_hi(ra[k], rb[k]), xp, acc[2 * k + 1]);
  }
}
__device__ __forceinline__ void lds_signal(unsigned* fl, int lane) {
  asm volatile("s_waitcnt lgkmcnt(0)" ::: "memory");
  if (lane == 0) *(lds_vu32*)fl = 1u;
}
__device__ __forceinline__ void lds_wait4(unsigned* fl4) {
#pragma unroll 1
  for (unsigned spin = 0; spin < (SPIN_CAP << 4); ++spin) {
    const v4u f = *(lds_vv4u*)fl4;
    if ((f[0] & f[1] & f[2] & f[3]) != 0u) break;
  }
  MEM_FENCE();
}
__device__ __forceinline__ unsigned now32() { return (unsigned)__builtin_amdgcn_s_memrealtime(); }
template <int SLEEP> __device__ __forceinline__ void wait_until(unsigned t) {
#pragma unroll 1
  for (int sp = 0; sp < 4096; ++sp) {
    if ((int)(now32() - t) >= 0) break;
    __builtin_amdgcn_s_sleep(SLEEP);
  }
  MEM_FENCE();
  SCHED_FENCE();
}
__device__ __forceinline__ void proj_part(const v4u* W, const v2u (&xp)[4], float* dst) {
  unsigned wp[16];
#pragma unroll
  for (int k = 0; k < 4; ++k) {
    wp[2 * k] = perm_lo(W[0][k], W[1][k]); wp[2 * k + 1] = perm_hi(W[0][k], W[1][k]);
    wp[8 + 2 * k] = perm_lo(W[2][k], W[3][k]); wp[8 + 2 * k + 1] = perm_hi(W[2][k], W[3][k]);
  }
  float t[8];
#pragma unroll
  for (int k = 0; k < 8; ++k) {
    const float a0 = dot2(wp[8 + k], xp[0][1], dot2(wp[k], xp[0][0], 0.f)), a1 = dot2(wp[8 + k], xp[1][1], dot2(wp[k], xp[1][0], 0.f));
    const float a2 = dot2(wp[8 + k], xp[2][1], dot2(wp[k], xp[2][0], 0.f)), a3 = dot2(wp[8 + k], xp[3][1], dot2(wp[k], xp[3][0], 0.f));
    t[k] = swap16_add(swap32_add(a0, a2), swap32_add(a1, a3));
  }
  *reinterpret_cast<f32x4*>(dst) = f32x4{t[0], t[1], t[2], t[3]};
  *reinterpret_cast<f32x4*>(dst + 4) = f32x4{t[4], t[5], t[6], t[7]};
}

template <bool VPASS = false>
__device__ __forceinline__ void ld_quarter(v4u* R, rsrc_t rk, rsrc_t rv, unsigned voff, int n, int rot, unsigned tb) {
  if (TAU256 > 0) {
    const unsigned due = tb + (unsigned)(SA_NS / 10) + (unsigned)((n * TAU256) >> 8) + (unsigned)((n < 12 ? n : 12) * (SB_NS / 10));
#pragma unroll 1
    for (int sp = 0; sp < 512; ++sp) {
      if ((int)(now32() - due) >= 0 || n >= 128) break;
      __builtin_amdgcn_s_sleep(1);
    }
    SCHED_FENCE();
  }
  const rsrc_t rn = n < 64 ? rk : rv;
  const unsigned off = n < 128 ? ((unsigned)(((n >> 1) + rot) & 31) << 15) + ((unsigned)(n & 1) << 12) : (1u << 20);
  if (AUX_K == AUX_V) {
#pragma unroll
    for (int i = 0; i < 4; ++i) R[i] = ld16<AUX_K>(rn, voff + i * 1024, off);
  } else if (VPASS) {
#pragma unroll
    for (int i = 0; i < 4; ++i) R[i] = ld16<AUX_V>(rn, voff + i * 1024, off);
  } else {
#pragma unroll
    for (int i = 0; i < 4; ++i) R[i] = ld16<AUX_K>(rn, voff + i * 1024, off);
  }
  SCHED_FENCE();
}
#define WAIT_Q() asm volatile("s_waitcnt vmcnt(4)" ::: "memory")
#define WAIT_PRO() asm volatile("s_waitcnt vmcnt(4)" ::: "memory")

template <bool REFILL, int SKIP = 0>
__device__ __forceinline__ float k_group(v4u* R, const v4u& q, int c, rsrc_t rk, rsrc_t rv, unsigned voff, int n0, int rot, unsigned tb) {
  float x[16];
#pragma unroll
  for (int k = 0; k < 4; ++k) {
    if (REFILL && k >= SKIP) {
      WAIT_Q();
      ld_quarter(R + 4 * ((k + DIST) & 3), rk, rv, voff, n0 + k + DIST, rot, tb);
    }
#pragma unroll
    for (int i = 4 * k; i < 4 * k + 4; ++i) {
      float d = dot2(R[i][0], q[0], 0.f); d = dot2(R[i][1], q[1], d); d = dot2(R[i][2], q[2], d); x[i] = dot2(R[i][3], q[3], d);
    }
    PIN4(x + 4 * k);
    SCHED_FENCE();
  }
#define TR_STEP(HALF, CTRL, BIT)                                                                 \
  _Pragma("unroll") for (int hh_ = 0; hh_ < (HALF); ++hh_) {                                     \
    const float lo_ = x[hh_], hi_ = x[hh_ + (HALF)];                                             \
    const float t1_ = lo_ + dpp<CTRL>(lo_), t2_ = hi_ + dpp<CTRL>(hi_);                          \
    x[hh_] = (c & (BIT)) ? t2_ : t1_;                                                            \
  }
  TR_STEP(8, XOR8, 8) TR_STEP(4, XOR7, 4) TR_STEP(2, XOR2, 2) TR_STEP(1, XOR1, 1)
#undef TR_STEP
  return rbf(rbf(x[0]) * SCALE);
}

template <int I> __device__ __forceinline__ void pv_one(float (&acc)[8], const v4u& V, float p) {
  const float pb = dpp<BCAST + I>(p);
#pragma unroll
  for (int k = 0; k < 4; ++k) {
    acc[2 * k] = fmaf(pb, bflo(V[k]), acc[2 * k]);
    acc[2 * k + 1] = fmaf(pb, bfhi(V[k]), acc[2 * k + 1]);
  }
}

#define STAMP(i) do { } while (0)

__global__ void __launch_bounds__(512, 2)
fused_decode_kernel(const u16* __restrict__ x, const u16* __restrict__ kc, const u16* __restrict__ vc, const u16* __restrict__ wq,
                    const u16* __restrict__ wo, const u16* __restrict__ wg, const u16* __restrict__ wu, const u16* __restrict__ wd,
                    u16* __restrict__ out, unsigned char* ws) {
  __shared__ __attribute__((aligned(16))) unsigned lds[L_END];
  float* ldsf = reinterpret_cast<float*>(lds);
  const int tid = threadIdx.x, lane = tid & 63;
  const int wave = __builtin_amdgcn_readfirstlane(tid >> 6) ^ 4;
  const int r = lane >> 4, c = lane & 15;
  const int bid = blockIdx.x, xg = bid & 7, idx = bid >> 3;
  const int hh = idx >> 3, h = xg + 8 * hh, b = (idx >> 1) & 3, s = idx & 1;
  const int bh = b * NH + h, u = h * 2 + s;
  const rsrc_t rws = mk_rsrc(ws, WS_END);
  const unsigned tb = now32();
  STAMP(0);

  if (wave < 4) {
    const size_t kvoff = ((size_t)bh * SK + (size_t)s * (SK / 2)) * DH;
    const rsrc_t rk = mk_rsrc(kc + kvoff, 1u << 20), rv = mk_rsrc(vc + kvoff, 1u << 20);
    const unsigned voff = lane * 16 + wave * 8192;
    const int rot = idx;
    v4u P[32], R[16];
    wg_barrier();
    ld_quarter(P, rk, rv, voff, 0, rot, tb);
    ld_quarter(P + 4, rk, rv, voff, 1, rot, tb);
#pragma unroll
    for (int k = 2; k < 8; ++k) { WAIT_PRO(); ld_quarter(P + 4 * k, rk, rv, voff, k, rot, tb); }
#pragma unroll
    for (int k = 0; k < 4; ++k) { WAIT_PRO(); ld_quarter(R + 4 * k, rk, rv, voff, 8 + k, rot, tb); }
    wg_barrier();
    STAMP(1);
    v4u q;
    {
      float qs[8];
#pragma unroll
      for (int k = 0; k < 8; ++k) qs[k] = 0.f;
#pragma unroll
      for (int w = 0; w < 4; ++w) {
        const f32x4 a0 = *reinterpret_cast<const f32x4*>(ldsf + L_QP + w * 128 + 8 * c);
        const f32x4 a1 = *reinterpret_cast<const f32x4*>(ldsf + L_QP + w * 128 + 8 * c + 4);
#pragma unroll
        for (int k = 0; k < 4; ++k) { qs[k] += a0[k]; qs[4 + k] += a1[k]; }
      }
#pragma unroll
      for (int k = 0; k < 4; ++k) q[k] = pk_bf16(qs[2 * k], qs[2 * k + 1]);
    }
    const unsigned tag = lds[L_TAG];
    float* sc = ldsf + L_SC + wave * 1024 + lane;
    float mx = k_group<true, 4 - DIST>(R, q, c, rk, rv, voff, 8, rot, tb);
    sc[2 * 64] = mx;
#pragma unroll
    for (int g = 0; g < 2; ++g) {
      const float sv = k_group<false>(P + 16 * g, q, c, rk, rv, voff, 0, rot, tb);
      sc[g * 64] = sv;
      mx = fmaxf(mx, sv);
    }
#pragma unroll 1
    for (int g = 3; g < 16; ++g) {
      const float sv = k_group<true>(R, q, c, rk, rv, voff, 4 * g, rot, tb);
      sc[g * 64] = sv;
      mx = fmaxf(mx, sv);
    }
    STAMP(2);
    mx = wave_max(mx);
    float l = 0.f;
#pragma unroll
    for (int g = 0; g < 16; ++g) {
      const float p = __builtin_amdgcn_exp2f((sc[g * 64] - mx) * LOG2E);
      sc[g * 64] = p;
      l += p;
    }
    l = wave_sum(l);
    __builtin_amdgcn_raw_buffer_store_b128(v4u{__float_as_uint(mx), tag, __float_as_uint(l), tag}, rws,
                                           lane == 0 ? WS_ML + (unsigned)((bh * 2 + s) * 4 + wave) * 16u : OOR, 0, AUX_SC1);
    STAMP(3);
    float acc[8];
#pragma unroll
    for (int k = 0; k < 8; ++k) acc[k] = 0.f;
#pragma unroll 1
    for (int g = 0; g < 16; ++g) {
      const float p = sc[g * 64];
      if (g == 10 && lane == 0) *(lds_vu32*)(lds + L_FL + 12 + wave) = 1u;
      const int n0 = 64 + 4 * g + DIST;
#define PV_Q(k)                                                                                  \
      WAIT_Q();                                                                                  \
      ld_quarter<true>(R + 4 * (((k) + DIST) & 3), rv, rv, voff, n0 + (k), rot, tb);             \
      pv_one<4 * (k)>(acc, R[4 * (k)], p); pv_one<4 * (k) + 1>(acc, R[4 * (k) + 1], p);          \
      pv_one<4 * (k) + 2>(acc, R[4 * (k) + 2], p); pv_one<4 * (k) + 3>(acc, R[4 * (k) + 3], p);  \
      PIN8(acc);                                                                                 \
      SCHED_FENCE();
      PV_Q(0) PV_Q(1) PV_Q(2) PV_Q(3)
#undef PV_Q
    }
    {
      float o0, o1;
      rows_reduce8(acc, o0, o1);
      *reinterpret_cast<f32x2*>(ldsf + L_OP + wave * 128 + 8 * c + 2 * r) = f32x2{o0, o1};
    }
    STAMP(4);
    wg_barrier();
    STAMP(5);
    if (wave != 0) return;
    const unsigned xw = *reinterpret_cast<const unsigned*>(x + b * DM + 8 * u + 2 * (lane & 3));
    const unsigned ybase = WS_Y + (unsigned)(b * 64 + u) * 4096u;
    v4u g0, g1, g2, g3;
#pragma unroll 1
    for (unsigned spin = 0;; ++spin) {
      MEM_FENCE();
      g0 = ld16<AUX_SC1>(rws, lane * 16, ybase);
      g1 = ld16<AUX_SC1>(rws, lane * 16 + 1024, ybase);
      g2 = ld16<AUX_SC1>(rws, lane * 16 + 2048, ybase);
      g3 = ld16<AUX_SC1>(rws, lane * 16 + 3072, ybase);
      const bool ok = g0[1] == tag && g0[3] == tag && g1[1] == tag && g1[3] == tag && g2[1] == tag && g2[3] == tag && g3[1] == tag && g3[3] == tag;
      if (__all(ok) || spin > SPIN_CAP) break;
      __builtin_amdgcn_s_sleep(8);
    }
    float y0 = (__uint_as_float(g0[0]) + __uint_as_float(g1[0])) + (__uint_as_float(g2[0]) + __uint_as_float(g3[0]));
    float y1 = (__uint_as_float(g0[2]) + __uint_as_float(g1[2])) + (__uint_as_float(g2[2]) + __uint_as_float(g3[2]));
    y0 += dpp<ROR4>(y0); y1 += dpp<ROR4>(y1);
    y0 += dpp<XOR8>(y0); y1 += dpp<XOR8>(y1);
    y0 = swap16_add(y0, y0); y1 = swap16_add(y1, y1);
    y0 = swap32_add(y0, y0); y1 = swap32_add(y1, y1);
    if (lane < 4) *reinterpret_cast<unsigned*>(out + b * DM + 8 * u + 2 * lane) = pk_bf16(bflo(xw) + y0, bfhi(xw) + y1);
    STAMP(6);
    if (u == 0 && b == 0 && lane == 0) __builtin_amdgcn_raw_buffer_store_b32(tag, rws, WS_EPOCH, 0, AUX_SC1);
  } else {
    const int hw = wave - 4;
    const v4u xr = ld16<0>(mk_rsrc(x + hw * DM, DM * 2), lane * 16, 0);
    const unsigned epoch = __builtin_amdgcn_raw_buffer_load_b32(rws, 0, WS_EPOCH, AUX_SC1);
    SCHED_FENCE();
    const int cq = xg * 32 + idx, pb = cq >> 3, kq = cq & 7;
    v4u W[32];
    {
      const rsrc_t rq = mk_rsrc(wq + (size_t)(64 * kq + 16 * hw) * (NH * DH) + pb * DH, 15 * 8192 + 256);
#pragma unroll
      for (int i = 0; i < 4; ++i) W[i] = ld16<AUX_NT>(rq, r * (4 * 8192) + c * 16, i * 8192);
      const u16* wf = pb < 14 ? wg + pb * 128 : wu + (pb < 28 ? pb - 14 : 0) * 128;
      const rsrc_t rf = mk_rsrc(wf + (size_t)(64 * kq + 16 * hw) * FF, (pb < 28 && !ABL_NOW) ? 15 * FF * 2 + 256 : 0);
#pragma unroll
      for (int i = 0; i < 4; ++i) W[4 + i] = ld16<AUX_NT>(rf, r * (4 * FF * 2) + c * 16, i * (FF * 2));
    }
    SCHED_FENCE();
    if (hw == 0 && lane < 16) lds[L_FL + lane] = 0u;
    wg_barrier();
    {
      float xv[8];
#pragma unroll
      for (int k = 0; k < 4; ++k) { xv[2 * k] = bflo(xr[k]); xv[2 * k + 1] = bfhi(xr[k]); }
      const float sm = ((xv[0] + xv[1]) + (xv[2] + xv[3])) + ((xv[4] + xv[5]) + (xv[6] + xv[7]));
      const float mean = wave_sum(sm) * (1.f / DM);
      float sq = 0.f;
#pragma unroll
      for (int k = 0; k < 8; ++k) { const float d = xv[k] - mean; sq = fmaf(d, d, sq); }
      const float var = wave_sum(sq) * (1.f / DM);
      const float mb = rbf(mean);
      const float sd = rbf(sqrtf(rbf(rbf(var) + 1e-5f)));
      const float ri = 1.f / sd;
      float xn[8];
#pragma unroll
      for (int k = 0; k < 8; ++k) {
        const float d = rbf(xv[k] - mb);
        const float q0 = d * ri;
        xn[k] = fmaf(fmaf(-q0, sd, d), ri, q0);
      }
      *reinterpret_cast<v4u*>(lds + L_XN + hw * 256 + 4 * lane) =
          v4u{pk_bf16(xn[0], xn[1]), pk_bf16(xn[2], xn[3]), pk_bf16(xn[4], xn[5]), pk_bf16(xn[6], xn[7])};
    }
    const unsigned tag = epoch + TAG_STEP;
    if (hw == 0 && lane == 0) lds[L_TAG] = tag;
    lds_signal(lds + L_FL + hw, lane);
    STAMP(1);
    lds_wait4(lds + L_FL);
    {
      v2u xp[4];
#pragma unroll
      for (int bb = 0; bb < 4; ++bb) xp[bb] = *reinterpret_cast<const v2u*>(lds + L_XN + bb * 256 + 32 * kq + 8 * hw + 2 * r);
      proj_part(W, xp, ldsf + L_SC + (hw * 4 + r) * 128 + 8 * c);
      proj_part(W + 4, xp, ldsf + L_SC + 2048 + (hw * 4 + r) * 128 + 8 * c);
      lds_signal(lds + L_FL + 4 + hw, lane);
      lds_wait4(lds + L_FL + 4);
      float q0 = 0.f, q1 = 0.f, f0 = 0.f, f1 = 0.f;
#pragma unroll
      for (int w = 0; w < 4; ++w) {
        const f32x2 tq = *reinterpret_cast<const f32x2*>(ldsf + L_SC + (w * 4 + hw) * 128 + 2 * lane);
        const f32x2 tf = *reinterpret_cast<const f32x2*>(ldsf + L_SC + 2048 + (w * 4 + hw) * 128 + 2 * lane);
        q0 += tq[0]; q1 += tq[1]; f0 += tf[0]; f1 += tf[1];
      }
      __builtin_amdgcn_raw_buffer_store_b128(v4u{__float_as_uint(q0), tag, __float_as_uint(q1), tag}, rws, lane * 16,
                                             WS_P + (unsigned)((hw * 60 + pb) * 8 + kq) * 1024u, AUX_SC1);
      __builtin_amdgcn_raw_buffer_store_b128(v4u{__float_as_uint(f0), tag, __float_as_uint(f1), tag}, rws, pb < 28 ? lane * 16 : OOR,
                                             WS_P + (unsigned)((hw * 60 + 32 + (pb < 28 ? pb : 0)) * 8 + kq) * 1024u, AUX_SC1);
    }
    {
      const unsigned qb = WS_P + (unsigned)((b * 60 + h) * 8 + 2 * hw) * 1024u;
      wait_until<4>(tb + QDELAY);
      v4u pa, pc;
#pragma unroll 1
      for (unsigned spin = 0;; ++spin) {
        MEM_FENCE();
        pa = ld16<AUX_SC1>(rws, lane * 16, qb);
        pc = ld16<AUX_SC1>(rws, lane * 16 + 1024, qb);
        if (__all(pa[1] == tag && pa[3] == tag && pc[1] == tag && pc[3] == tag) || spin > SPIN_CAP) break;
        __builtin_amdgcn_s_sleep(8);
      }
      *reinterpret_cast<f32x2*>(ldsf + L_QP + hw * 128 + 2 * lane) =
          f32x2{__uint_as_float(pa[0]) + __uint_as_float(pc[0]), __uint_as_float(pa[2]) + __uint_as_float(pc[2])};
    }
    STAMP(2);
    wg_barrier();
    unsigned hp;
    {
      const int seg = lane / 14, piece = lane - seg * 14;
      const int col = 28 * u + 2 * piece, kk = 2 * hw + (seg & 1), mat = seg >> 1;
      const unsigned o = lane < 56 ? WS_P + (unsigned)(((b * 60 + 32 + 14 * mat + (col >> 7)) * 8 + kk) * 128 + (col & 127)) * 8u : OOR;
      v4u gu;
#pragma unroll 1
      for (unsigned spin = 0;; ++spin) {
        MEM_FENCE();
        gu = ld16<AUX_SC1>(rws, o, 0);
        if (__all(lane >= 56 || (gu[1] == tag && gu[3] == tag)) || spin > SPIN_CAP || ABL_NOW) break;
        __builtin_amdgcn_s_sleep(64);
      }
      if (lane < 56) *reinterpret_cast<v2u*>(lds + L_GU + (kk * 2 + mat) * 32 + 2 * piece) = v2u{gu[0], gu[2]};
      lds_signal(lds + L_FL + 8 + hw, lane);
      lds_wait4(lds + L_FL + 8);
      const int j = 8 * r + (c & 7);
      float gg = 0.f, uu = 0.f;
#pragma unroll
      for (int k8 = 0; k8 < 8; ++k8) { gg += ldsf[L_GU + k8 * 64 + j]; uu += ldsf[L_GU + k8 * 64 + 32 + j]; }
      gg = rbf(gg); uu = rbf(uu);
      const float sg = rbf(gg / (1.f + __expf(-gg)));
      const float hv = j < 28 ? sg * uu : 0.f;
      hp = pk_bf16(hv, dpp<XOR1>(hv));
      asm volatile("" : "+v"(hp));
    }
    const unsigned wphase = (unsigned)hh * WIN_PHASE;
    const unsigned cyc0 = ((((tb + WIN_START + wphase) >> (WIN_SHIFT + 1)) + 1u) << (WIN_SHIFT + 1)) - wphase;
    float y[8];
#pragma unroll
    for (int k = 0; k < 8; ++k) y[k] = 0.f;
    {
      const rsrc_t rd = mk_rsrc(wd + (size_t)(28 * u) * DM + 128 * hw, ABL_NOW ? 0 : 27 * 1024 + 256);
#define DSTEP(J)                                                                                 \
      {                                                                                          \
        wait_until<2>(cyc0 + ((unsigned)(J) << WIN_SHIFT));                                      \
        const v4u d0 = ld16<AUX_NT>(rd, r * (8 * 1024) + c * 16, (2 * (J)) * 1024);              \
        const v4u d1 = ld16<AUX_NT>(rd, r * (8 * 1024) + c * 16, (2 * (J) + 1) * 1024);          \
        gemv_pair(y, d0, d1, dppu<BCAST + 2 * (J)>(hp));                                         \
        PIN8(y);                                                                                 \
      }
      DSTEP(0) DSTEP(1) DSTEP(2) DSTEP(3)
#undef DSTEP
      const rsrc_t ro = mk_rsrc(wo + (size_t)(h * DH) * DM + 128 * hw, ABL_NOW ? 0 : 127 * 1024 + 256);
#pragma unroll
      for (int j = 0; j < 16; ++j) {
        wait_until<2>(cyc0 + ((unsigned)(4 + j) << WIN_SHIFT));
        const v4u w0 = ld16<AUX_NT>(ro, r * (32 * 1024) + c * 16, (2 * j) * 1024);
        const v4u w1 = ld16<AUX_NT>(ro, r * (32 * 1024) + c * 16, (2 * j + 1) * 1024);
#pragma unroll
        for (int k = 0; k < 4; ++k) { W[2 * j][k] = perm_lo(w0[k], w1[k]); W[2 * j + 1][k] = perm_hi(w0[k], w1[k]); }
        asm volatile("" : "+v"(W[2 * j]), "+v"(W[2 * j + 1]) :: "memory");
      }
    }
    STAMP(3);
    float f[4];
    {
#pragma unroll 1
      for (unsigned spin = 0; spin < SPIN_CAP; ++spin) {
        if (*(lds_vu32*)(lds + L_FL + 12 + hw) != 0u) break;
        __builtin_amdgcn_s_sleep(32);
      }
      const unsigned o = lane < 8 ? WS_ML + (unsigned)(bh * 8 + lane) * 16u : OOR;
      v4u ml;
#pragma unroll 1
      for (unsigned spin = 0;; ++spin) {
        MEM_FENCE();
        ml = ld16<AUX_SC1>(rws, o, 0);
        if (__all(lane >= 8 || (ml[1] == tag && ml[3] == tag)) || spin > SPIN_CAP) break;
        __builtin_amdgcn_s_sleep(64);
      }
      const float mw = lane < 8 ? __uint_as_float(ml[0]) : -INFINITY;
      float M = fmaxf(mw, dpp<XOR1>(mw)); M = fmaxf(M, dpp<XOR2>(M)); M = fmaxf(M, dpp<XOR7>(M));
      const float wt = lane < 8 ? __builtin_amdgcn_exp2f((mw - M) * LOG2E) : 0.f;
      float L = wt * (lane < 8 ? __uint_as_float(ml[2]) : 0.f);
      L += dpp<XOR1>(L); L += dpp<XOR2>(L); L += dpp<XOR7>(L);
      const float fw = wt / L;
#pragma unroll
      for (int w = 0; w < 4; ++w) f[w] = __uint_as_float(__builtin_amdgcn_readlane(__float_as_uint(fw), 4 * s + w));
      PIN8(y);
    }
    STAMP(4);
    wg_barrier();
    STAMP(5);
    {
      float o0 = 0.f, o1 = 0.f;
#pragma unroll
      for (int w = 0; w < 4; ++w) {
        const f32x2 t = *reinterpret_cast<const f32x2*>(ldsf + L_OP + w * 128 + 32 * r + 2 * c);
        o0 = fmaf(f[w], t[0], o0); o1 = fmaf(f[w], t[1], o1);
      }
      const unsigned opk = pk_bf16(o0, o1);
#define YSTEP(J)                                                                                 \
  {                                                                                              \
    const unsigned ob = dppu<BCAST + (J)>(opk);                                                  \
    _Pragma("unroll") for (int k = 0; k < 4; ++k) {                                              \
      y[2 * k] = dot2(W[2 * (J)][k], ob, y[2 * k]);                                              \
      y[2 * k + 1] = dot2(W[2 * (J) + 1][k], ob, y[2 * k + 1]);                                  \
    }                                                                                            \
  }
      YSTEP(0) YSTEP(1) YSTEP(2) YSTEP(3) YSTEP(4) YSTEP(5) YSTEP(6) YSTEP(7)
      YSTEP(8) YSTEP(9) YSTEP(10) YSTEP(11) YSTEP(12) YSTEP(13) YSTEP(14) YSTEP(15)
#undef YSTEP
      float y0, y1;
      rows_reduce8(y, y0, y1);
      const unsigned o = WS_Y + (unsigned)(b * 64 + 16 * hw + c) * 4096u + (unsigned)u * 64u + (unsigned)r * 16u;
      __builtin_amdgcn_raw_buffer_store_b128(v4u{__float_as_uint(y0), tag, __float_as_uint(y1), tag}, rws, o, 0, AUX_SC1);
      STAMP(6);
    }
  }
}

}

extern "C" void kernel_launch(void* const* d_in, const int* in_sizes, int n_in, void* d_out, int out_size, void* d_ws, size_t ws_size,
                              hipStream_t stream) {
  (void)in_sizes; (void)n_in; (void)out_size; (void)ws_size;
  hipLaunchKernelGGL(fused_decode_kernel, dim3(256), dim3(512), 0, stream, (const u16*)d_in[0], (const u16*)d_in[1], (const u16*)d_in[2],
                     (const u16*)d_in[3], (const u16*)d_in[4], (const u16*)d_in[6], (const u16*)d_in[7], (const u16*)d_in[8], (u16*)d_out,
                     (unsigned char*)d_ws);
}
```

```cpp
#define TAU_NS 2290
#include <hip/hip_runtime.h>
#include <hip/hip_bf16.h>
#include <stdint.h>

typedef unsigned short u16;
typedef unsigned v4u __attribute__((ext_vector_type(4)));
typedef unsigned v2u __attribute__((ext_vector_type(2)));
typedef float f32x4 __attribute__((ext_vector_type(4)));
typedef float f32x2 __attribute__((ext_vector_type(2)));
typedef __bf16 bf16x2 __attribute__((ext_vector_type(2)));
typedef __amdgpu_buffer_rsrc_t rsrc_t;
typedef __attribute__((address_space(3))) volatile unsigned lds_vu32;
typedef __attribute__((address_space(3))) volatile v4u lds_vv4u;

namespace {

constexpr int DM = 512, NH = 32, SK = 8192, DH = 128, FF = 1792;
constexpr float SCALE = 0.08838834764831845f;
constexpr float LOG2E = 1.4426950408889634f;
constexpr int AUX_NT = 2;
constexpr int AUX_SC1 = 16;
constexpr unsigned TAG_STEP = 0x9E3779B9u;
constexpr unsigned SPIN_CAP = 1u << 18;
constexpr unsigned OOR = 0x7ffffff0u;

#define DIST 2
#define SA_NS 900
#define SB_NS 100
constexpr int TAU256 = TAU_NS * 256 / 40;
#define QDELAY 450
#define LAY 1
#define PH_MODE 5
#define COMMON_ORG 1
#define YDELAY 115
#define YSLEEP 4
#define WIN_SHIFT 8
#define WIN_PHASE 64
#define WIN_START 1000
#define AUX_K 2
#define AUX_V 2
#define ABL_NOW 0

constexpr unsigned WS_EPOCH = 0;
constexpr unsigned WS_T0 = 8192;
constexpr unsigned WS_ML = 16384;
constexpr unsigned WS_P = WS_ML + 128 * 128;
constexpr unsigned WS_Y = WS_P + 4 * 60 * 8 * 1024;
constexpr unsigned WS_END = WS_Y + 4 * 64 * 64 * 64;

constexpr int L_SC = 0;
constexpr int L_XN = L_SC + 4096;
constexpr int L_QP = L_XN + 1024;
constexpr int L_OP = L_QP + 512;
constexpr int L_GU = L_OP + 512;
constexpr int L_FL = L_GU + 512;
constexpr int L_MX = L_FL + 20;
constexpr int L_TAG = L_MX + 4;
constexpr int L_END = L_TAG + 4;

__device__ __forceinline__ rsrc_t mk_rsrc(const void* p, unsigned bytes) {
  return __builtin_amdgcn_make_buffer_rsrc(const_cast<void*>(p), 0, bytes, 0x00020000);
}
template <int AUX> __device__ __forceinline__ v4u ld16(rsrc_t r, unsigned voff, unsigned soff) {
  return __builtin_amdgcn_raw_buffer_load_b128(r, voff, soff, AUX);
}
__device__ __forceinline__ float dot2(unsigned a, unsigned b, float acc) {
  return __builtin_amdgcn_fdot2_f32_bf16(__builtin_bit_cast(bf16x2, a), __builtin_bit_cast(bf16x2, b), acc, false);
}
__device__ __forceinline__ float bflo(unsigned w) { return __uint_as_float(w << 16); }
__device__ __forceinline__ float bfhi(unsigned w) { return __uint_as_float(w & 0xffff0000u); }
__device__ __forceinline__ unsigned pk_bf16(float lo, float hi) {
  bf16x2 v = {(__bf16)lo, (__bf16)hi};
  return __builtin_bit_cast(unsigned, v);
}
__device__ __forceinline__ float rbf(float x) { return (float)(__bf16)x; }
template <int CTRL> __device__ __forceinline__ float dpp(float x) {
  return __builtin_bit_cast(float, __builtin_amdgcn_mov_dpp(__builtin_bit_cast(int, x), CTRL, 0xf, 0xf, true));
}
template <int CTRL> __device__ __forceinline__ unsigned dppu(unsigned x) {
  return (unsigned)__builtin_amdgcn_mov_dpp((int)x, CTRL, 0xf, 0xf, true);
}
constexpr int XOR1 = 0xB1, XOR2 = 0x4E, XOR7 = 0x141, XOR8 = 0x128, ROR4 = 0x124, BCAST = 0x150;
__device__ __forceinline__ float swap32_add(float a, float b) {
  auto t = __builtin_amdgcn_permlane32_swap(__float_as_uint(a), __float_as_uint(b), false, false);
  return __uint_as_float(t[0]) + __uint_as_float(t[1]);
}
__device__ __forceinline__ float swap16_add(float a, float b) {
  auto t = __builtin_amdgcn_permlane16_swap(__float_as_uint(a), __float_as_uint(b), false, false);
  return __uint_as_float(t[0]) + __uint_as_float(t[1]);
}
__device__ __forceinline__ float wave_sum(float x) {
  x += dpp<XOR1>(x); x += dpp<XOR2>(x); x += dpp<XOR7>(x); x += dpp<XOR8>(x);
  x = swap16_add(x, x); x = swap32_add(x, x);
  return x;
}
__device__ __forceinline__ float wave_max(float x) {
  x = fmaxf(x, dpp<XOR1>(x)); x = fmaxf(x, dpp<XOR2>(x)); x = fmaxf(x, dpp<XOR7>(x)); x = fmaxf(x, dpp<XOR8>(x));
  auto s = __builtin_amdgcn_permlane16_swap(__float_as_uint(x), __float_as_uint(x), false, false);
  x = fmaxf(__uint_as_float(s[0]), __uint_as_float(s[1]));
  auto t = __builtin_amdgcn_permlane32_swap(__float_as_uint(x), __float_as_uint(x), false, false);
  return fmaxf(__uint_as_float(t[0]), __uint_as_float(t[1]));
}
__device__ __forceinline__ void rows_reduce8(const float (&a)[8], float& o0, float& o1) {
  const float t0 = swap32_add(a[0], a[4]), t1 = swap32_add(a[1], a[5]), t2 = swap32_add(a[2], a[6]), t3 = swap32_add(a[3], a[7]);
  o0 = swap16_add(t0, t2); o1 = swap16_add(t1, t3);
}
__device__ __forceinline__ void wg_barrier() { asm volatile("s_waitcnt lgkmcnt(0)\n\ts_barrier" ::: "memory"); }
#define SCHED_FENCE() __builtin_amdgcn_sched_barrier(0)
#define MEM_FENCE() asm volatile("" ::: "memory")
#define PIN4(a) asm volatile("" : "+v"((a)[0]), "+v"((a)[1]), "+v"((a)[2]), "+v"((a)[3]) :: "memory")
#define PIN8(a) asm volatile("" : "+v"((a)[0]), "+v"((a)[1]), "+v"((a)[2]), "+v"((a)[3]), "+v"((a)[4]), "+v"((a)[5]), "+v"((a)[6]), "+v"((a)[7]) :: "memory")
__device__ __forceinline__ unsigned perm_lo(unsigned a, unsigned b) { return __builtin_amdgcn_perm(b, a, 0x05040100u); }
__device__ __forceinline__ unsigned perm_hi(unsigned a, unsigned b) { return __builtin_amdgcn_perm(b, a, 0x07060302u); }
__device__ __forceinline__ void gemv_pair(float (&acc)[8], const v4u& ra, const v4u& rb, unsigned xp) {
#pragma unroll
  for (int k = 0; k < 4; ++k) {
    acc[2 * k] = dot2(perm_lo(ra[k], rb[k]), xp, acc[2 * k]);
    acc[2 * k + 1] = dot2(perm_hi(ra[k], rb[k]), xp, acc[2 * k + 1]);
  }
}
__device__ __forceinline__ void lds_signal(unsigned* fl, int lane) {
  asm volatile("s_waitcnt lgkmcnt(0)" ::: "memory");
  if (lane == 0) *(lds_vu32*)fl = 1u;
}
__device__ __forceinline__ void lds_wait4(unsigned* fl4) {
#pragma unroll 1
  for (unsigned spin = 0; spin < (SPIN_CAP << 4); ++spin) {
    const v4u f = *(lds_vv4u*)fl4;
    if ((f[0] & f[1] & f[2] & f[3]) != 0u) break;
  }
  MEM_FENCE();
}
__device__ __forceinline__ unsigned now32() { return (unsigned)__builtin_amdgcn_s_memrealtime(); }
template <int SLEEP> __device__ __forceinline__ void wait_until(unsigned t) {
#pragma unroll 1
  for (int sp = 0; sp < 4096; ++sp) {
    if ((int)(now32() - t) >= 0) break;
    __builtin_amdgcn_s_sleep(SLEEP);
  }
  MEM_FENCE();
  SCHED_FENCE();
}
__device__ __forceinline__ void proj_part(const v4u* W, const v2u (&xp)[4], float* dst) {
  unsigned wp[16];
#pragma unroll
  for (int k = 0; k < 4; ++k) {
    wp[2 * k] = perm_lo(W[0][k], W[1][k]); wp[2 * k + 1] = perm_hi(W[0][k], W[1][k]);
    wp[8 + 2 * k] = perm_lo(W[2][k], W[3][k]); wp[8 + 2 * k + 1] = perm_hi(W[2][k], W[3][k]);
  }
  float t[8];
#pragma unroll
  for (int k = 0; k < 8; ++k) {
    const float a0 = dot2(wp[8 + k], xp[0][1], dot2(wp[k], xp[0][0], 0.f)), a1 = dot2(wp[8 + k], xp[1][1], dot2(wp[k], xp[1][0], 0.f));
    const float a2 = dot2(wp[8 + k], xp[2][1], dot2(wp[k], xp[2][0], 0.f)), a3 = dot2(wp[8 + k], xp[3][1], dot2(wp[k], xp[3][0], 0.f));
    t[k] = swap16_add(swap32_add(a0, a2), swap32_add(a1, a3));
  }
  *reinterpret_cast<f32x4*>(dst) = f32x4{t[0], t[1], t[2], t[3]};
  *reinterpret_cast<f32x4*>(dst + 4) = f32x4{t[4], t[5], t[6], t[7]};
}

template <bool VPASS = false>
__device__ __forceinline__ void ld_quarter(v4u* R, rsrc_t rk, rsrc_t rv, unsigned voff, int n, int rot, unsigned tb) {
  if (TAU256 > 0) {
    const unsigned due = tb + (unsigned)(SA_NS / 10) + (unsigned)((n * TAU256) >> 8) + (unsigned)((n < 12 ? n : 12) * (SB_NS / 10));
#pragma unroll 1
    for (int sp = 0; sp < 512; ++sp) {
      if ((int)(now32() - due) >= 0 || n >= 128) break;
      __builtin_amdgcn_s_sleep(1);
    }
    SCHED_FENCE();
  }
  const rsrc_t rn = n < 64 ? rk : rv;
  const unsigned nr = (unsigned)(n + rot) & 63u;
  const unsigned off = n < 128 ? (LAY == 1 ? nr << 14 : ((nr >> 1) << 15) + ((nr & 1u) << 12)) : (1u << 20);
  if (AUX_K == AUX_V) {
#pragma unroll
    for (int i = 0; i < 4; ++i) R[i] = ld16<AUX_K>(rn, voff + i * 1024, off);
  } else if (VPASS) {
#pragma unroll
    for (int i = 0; i < 4; ++i) R[i] = ld16<AUX_V>(rn, voff + i * 1024, off);
  } else {
#pragma unroll
    for (int i = 0; i < 4; ++i) R[i] = ld16<AUX_K>(rn, voff + i * 1024, off);
  }
  SCHED_FENCE();
}
#define WAIT_Q() asm volatile("s_waitcnt vmcnt(4)" ::: "memory")
#define WAIT_PRO() asm volatile("s_waitcnt vmcnt(4)" ::: "memory")

template <bool REFILL, int SKIP = 0>
__device__ __forceinline__ float k_group(v4u* R, const v4u& q, int c, rsrc_t rk, rsrc_t rv, unsigned voff, int n0, int rot, unsigned tb) {
  float x[16];
#pragma unroll
  for (int k = 0; k < 4; ++k) {
    if (REFILL && k >= SKIP) {
      WAIT_Q();
      ld_quarter(R + 4 * ((k + DIST) & 3), rk, rv, voff, n0 + k + DIST, rot, tb);
    }
#pragma unroll
    for (int i = 4 * k; i < 4 * k + 4; ++i) {
      float d = dot2(R[i][0], q[0], 0.f); d = dot2(R[i][1], q[1], d); d = dot2(R[i][2], q[2], d); x[i] = dot2(R[i][3], q[3], d);
    }
    PIN4(x + 4 * k);
    SCHED_FENCE();
  }
#define TR_STEP(HALF, CTRL, BIT)                                                                 \
  _Pragma("unroll") for (int hh_ = 0; hh_ < (HALF); ++hh_) {                                     \
    const float lo_ = x[hh_], hi_ = x[hh_ + (HALF)];                                             \
    const float t1_ = lo_ + dpp<CTRL>(lo_), t2_ = hi_ + dpp<CTRL>(hi_);                          \
    x[hh_] = (c & (BIT)) ? t2_ : t1_;                                                            \
  }
  TR_STEP(8, XOR8, 8) TR_STEP(4, XOR7, 4) TR_STEP(2, XOR2, 2) TR_STEP(1, XOR1, 1)
#undef TR_STEP
  return rbf(rbf(x[0]) * SCALE);
}

template <int I> __device__ __forceinline__ void pv_one(float (&acc)[8], const v4u& V, float p) {
  const float pb = dpp<BCAST + I>(p);
#pragma unroll
  for (int k = 0; k < 4; ++k) {
    acc[2 * k] = fmaf(pb, bflo(V[k]), acc[2 * k]);
    acc[2 * k + 1] = fmaf(pb, bfhi(V[k]), acc[2 * k + 1]);
  }
}

#define STAMP(i) do { } while (0)

__global__ void __launch_bounds__(512, 2)
fused_decode_kernel(const u16* __restrict__ x, const u16* __restrict__ kc, const u16* __restrict__ vc, const u16* __restrict__ wq,
                    const u16* __restrict__ wo, const u16* __restrict__ wg, const u16* __restrict__ wu, const u16* __restrict__ wd,
                    u16* __restrict__ out, unsigned char* ws) {
  __shared__ __attribute__((aligned(16))) unsigned lds[L_END];
  float* ldsf = reinterpret_cast<float*>(lds);
  const int tid = threadIdx.x, lane = tid & 63;
  const int wave = __builtin_amdgcn_readfirstlane(tid >> 6) ^ 4;
  const int r = lane >> 4, c = lane & 15;
  const int bid = blockIdx.x, xg = bid & 7, idx = bid >> 3;
  const int hh = idx >> 3, h = xg + 8 * hh, b = (idx >> 1) & 3, s = idx & 1;
  const int bh = b * NH + h, u = h * 2 + s;
  const rsrc_t rws = mk_rsrc(ws, WS_END);
  const unsigned tb = now32();
  STAMP(0);

  if (wave < 4) {
    const size_t kvoff = ((size_t)bh * SK + (size_t)s * (SK / 2)) * DH;
    const rsrc_t rk = mk_rsrc(kc + kvoff, 1u << 20), rv = mk_rsrc(vc + kvoff, 1u << 20);
    const unsigned voff = lane * 16 + wave * (LAY == 1 ? 4096 : 8192);
    const int rot = bh * 2 + s;
    v4u P[32], R[16];
    wg_barrier();
    ld_quarter(P, rk, rv, voff, 0, rot, tb);
    ld_quarter(P + 4, rk, rv, voff, 1, rot, tb);
#pragma unroll
    for (int k = 2; k < 8; ++k) { WAIT_PRO(); ld_quarter(P + 4 * k, rk, rv, voff, k, rot, tb); }
#pragma unroll
    for (int k = 0; k < 4; ++k) { WAIT_PRO(); ld_quarter(R + 4 * k, rk, rv, voff, 8 + k, rot, tb); }
    wg_barrier();
    STAMP(1);
    v4u q;
    {
      float qs[8];
#pragma unroll
      for (int k = 0; k < 8; ++k) qs[k] = 0.f;
#pragma unroll
      for (int w = 0; w < 4; ++w) {
        const f32x4 a0 = *reinterpret_cast<const f32x4*>(ldsf + L_QP + w * 128 + 8 * c);
        const f32x4 a1 = *reinterpret_cast<const f32x4*>(ldsf + L_QP + w * 128 + 8 * c + 4);
#pragma unroll
        for (int k = 0; k < 4; ++k) { qs[k] += a0[k]; qs[4 + k] += a1[k]; }
      }
#pragma unroll
      for (int k = 0; k < 4; ++k) q[k] = pk_bf16(qs[2 * k], qs[2 * k + 1]);
    }
    const unsigned tag = lds[L_TAG];
    const int ph32 = PH_MODE == 5 ? 4 * ((idx + xg) & 7) : PH_MODE == 6 ? ((idx + 4 * xg) & 31) : PH_MODE == 7 ? ((4 * idx + (idx >> 3) + 4 * xg) & 31) : ((idx * 8 + xg) >> 3 & 31);
    const unsigned tc = __builtin_amdgcn_readfirstlane(lds[L_TAG + 1]) - (unsigned)((ph32 * TAU256) >> 13);
    float* sc = ldsf + L_SC + wave * 1024 + lane;
    float mx = k_group<true, 4 - DIST>(R, q, c, rk, rv, voff, 8, rot, tc);
    sc[2 * 64] = mx;
    {
      const float sv = k_group<false>(P, q, c, rk, rv, voff, 0, rot, tc);
      sc[0] = sv;
      mx = fmaxf(mx, sv);
    }
#pragma unroll 1
    for (int g = 3; g < 16; ++g) {
      const float sv = k_group<true>(R, q, c, rk, rv, voff, 4 * g, rot, tc);
      sc[g * 64] = sv;
      mx = fmaxf(mx, sv);
      if (g == 3) {
        const float sw = k_group<false>(P + 16, q, c, rk, rv, voff, 0, rot, tc);
        sc[64] = sw;
        mx = fmaxf(mx, sw);
      }
    }
    STAMP(2);
    mx = wave_max(mx);
    if (lane == 0) ldsf[L_MX + wave] = mx;
    lds_signal(lds + L_FL + 16 + wave, lane);
    STAMP(3);
    float acc[8];
#pragma unroll
    for (int k = 0; k < 8; ++k) acc[k] = 0.f;
#pragma unroll 1
    for (int g = 0; g < 16; ++g) {
      const float p = __builtin_amdgcn_exp2f((sc[g * 64] - mx) * LOG2E);
      if (g == 10 && lane == 0) *(lds_vu32*)(lds + L_FL + 12 + wave) = 1u;
      const int n0 = 64 + 4 * g + DIST;
#define PV_Q(k)                                                                                  \
      WAIT_Q();                                                                                  \
      ld_quarter<true>(R + 4 * (((k) + DIST) & 3), rv, rv, voff, n0 + (k), rot, tc);             \
      pv_one<4 * (k)>(acc, R[4 * (k)], p); pv_one<4 * (k) + 1>(acc, R[4 * (k) + 1], p);          \
      pv_one<4 * (k) + 2>(acc, R[4 * (k) + 2], p); pv_one<4 * (k) + 3>(acc, R[4 * (k) + 3], p);  \
      PIN8(acc);                                                                                 \
      SCHED_FENCE();
      PV_Q(0) PV_Q(1) PV_Q(2) PV_Q(3)
#undef PV_Q
    }
    {
      float o0, o1;
      rows_reduce8(acc, o0, o1);
      *reinterpret_cast<f32x2*>(ldsf + L_OP + wave * 128 + 8 * c + 2 * r) = f32x2{o0, o1};
    }
    STAMP(4);
    wg_barrier();
    STAMP(5);
    if (wave != 0) return;
    const unsigned xw = *reinterpret_cast<const unsigned*>(x + b * DM + 8 * u + 2 * (lane & 3));
    const unsigned ybase = WS_Y + (unsigned)(b * 64 + u) * 4096u;
    wait_until<1>(now32() + YDELAY);
    v4u g0, g1, g2, g3;
#pragma unroll 1
    for (unsigned spin = 0;; ++spin) {
      MEM_FENCE();
      g0 = ld16<AUX_SC1>(rws, lane * 16, ybase);
      g1 = ld16<AUX_SC1>(rws, lane * 16 + 1024, ybase);
      g2 = ld16<AUX_SC1>(rws, lane * 16 + 2048, ybase);
      g3 = ld16<AUX_SC1>(rws, lane * 16 + 3072, ybase);
      const bool ok = g0[1] == tag && g0[3] == tag && g1[1] == tag && g1[3] == tag && g2[1] == tag && g2[3] == tag && g3[1] == tag && g3[3] == tag;
      if (__all(ok) || spin > SPIN_CAP) break;
      __builtin_amdgcn_s_sleep(YSLEEP);
    }
    float y0 = (__uint_as_float(g0[0]) + __uint_as_float(g1[0])) + (__uint_as_float(g2[0]) + __uint_as_float(g3[0]));
    float y1 = (__uint_as_float(g0[2]) + __uint_as_float(g1[2])) + (__uint_as_float(g2[2]) + __uint_as_float(g3[2]));
    y0 += dpp<ROR4>(y0); y1 += dpp<ROR4>(y1);
    y0 += dpp<XOR8>(y0); y1 += dpp<XOR8>(y1);
    y0 = swap16_add(y0, y0); y1 = swap16_add(y1, y1);
    y0 = swap32_add(y0, y0); y1 = swap32_add(y1, y1);
    if (lane < 4) *reinterpret_cast<unsigned*>(out + b * DM + 8 * u + 2 * lane) = pk_bf16(bflo(xw) + y0, bfhi(xw) + y1);
    STAMP(6);
    if (u == 0 && b == 0 && lane == 0) __builtin_amdgcn_raw_buffer_store_b32(tag, rws, WS_EPOCH, 0, AUX_SC1);
  } else {
    const int hw = wave - 4;
    const v4u xr = ld16<0>(mk_rsrc(x + hw * DM, DM * 2), lane * 16, 0);
    const unsigned epoch = __builtin_amdgcn_raw_buffer_load_b32(rws, 0, WS_EPOCH, AUX_SC1);
    SCHED_FENCE();
    const int cq = xg * 32 + idx, pb = cq >> 3, kq = cq & 7;
    v4u W[32];
    {
      const rsrc_t rq = mk_rsrc(wq + (size_t)(64 * kq + 16 * hw) * (NH * DH) + pb * DH, 15 * 8192 + 256);
#pragma unroll
      for (int i = 0; i < 4; ++i) W[i] = ld16<AUX_NT>(rq, r * (4 * 8192) + c * 16, i * 8192);
      const u16* wf = pb < 14 ? wg + pb * 128 : wu + (pb < 28 ? pb - 14 : 0) * 128;
      const rsrc_t rf = mk_rsrc(wf + (size_t)(64 * kq + 16 * hw) * FF, (pb < 28 && !ABL_NOW) ? 15 * FF * 2 + 256 : 0);
#pragma unroll
      for (int i = 0; i < 4; ++i) W[4 + i] = ld16<AUX_NT>(rf, r * (4 * FF * 2) + c * 16, i * (FF * 2));
    }
    SCHED_FENCE();
    if (hw == 0 && lane < 20) lds[L_FL + lane] = 0u;
    wg_barrier();
    {
      float xv[8];
#pragma unroll
      for (int k = 0; k < 4; ++k) { xv[2 * k] = bflo(xr[k]); xv[2 * k + 1] = bfhi(xr[k]); }
      const float sm = ((xv[0] + xv[1]) + (xv[2] + xv[3])) + ((xv[4] + xv[5]) + (xv[6] + xv[7]));
      const float mean = wave_sum(sm) * (1.f / DM);
      float sq = 0.f;
#pragma unroll
      for (int k = 0; k < 8; ++k) { const float d = xv[k] - mean; sq = fmaf(d, d, sq); }
      const float var = wave_sum(sq) * (1.f / DM);
      const float mb = rbf(mean);
      const float sd = rbf(sqrtf(rbf(rbf(var) + 1e-5f)));
      const float ri = 1.f / sd;
      float xn[8];
#pragma unroll
      for (int k = 0; k < 8; ++k) {
        const float d = rbf(xv[k] - mb);
        const float q0 = d * ri;
        xn[k] = fmaf(fmaf(-q0, sd, d), ri, q0);
      }
      *reinterpret_cast<v4u*>(lds + L_XN + hw * 256 + 4 * lane) =
          v4u{pk_bf16(xn[0], xn[1]), pk_bf16(xn[2], xn[3]), pk_bf16(xn[4], xn[5]), pk_bf16(xn[6], xn[7])};
    }
    const unsigned tag = epoch + TAG_STEP;
    if (hw == 0 && lane == 0) lds[L_TAG] = tag;
    if (COMMON_ORG && hw == 1 && idx == 0)
      __builtin_amdgcn_raw_buffer_store_b64(v2u{tb, tag}, rws, lane < 32 ? WS_T0 + (unsigned)lane * 128u + (unsigned)xg * 8u : OOR, 0, AUX_SC1);
    lds_signal(lds + L_FL + hw, lane);
    STAMP(1);
    lds_wait4(lds + L_FL);
    {
      v2u xp[4];
#pragma unroll
      for (int bb = 0; bb < 4; ++bb) xp[bb] = *reinterpret_cast<const v2u*>(lds + L_XN + bb * 256 + 32 * kq + 8 * hw + 2 * r);
      proj_part(W, xp, ldsf + L_SC + (hw * 4 + r) * 128 + 8 * c);
      proj_part(W + 4, xp, ldsf + L_SC + 2048 + (hw * 4 + r) * 128 + 8 * c);
      lds_signal(lds + L_FL + 4 + hw, lane);
      lds_wait4(lds + L_FL + 4);
      float q0 = 0.f, q1 = 0.f, f0 = 0.f, f1 = 0.f;
#pragma unroll
      for (int w = 0; w < 4; ++w) {
        const f32x2 tq = *reinterpret_cast<const f32x2*>(ldsf + L_SC + (w * 4 + hw) * 128 + 2 * lane);
        const f32x2 tf = *reinterpret_cast<const f32x2*>(ldsf + L_SC + 2048 + (w * 4 + hw) * 128 + 2 * lane);
        q0 += tq[0]; q1 += tq[1]; f0 += tf[0]; f1 += tf[1];
      }
      __builtin_amdgcn_raw_buffer_store_b128(v4u{__float_as_uint(q0), tag, __float_as_uint(q1), tag}, rws, lane * 16,
                                             WS_P + (unsigned)((hw * 60 + pb) * 8 + kq) * 1024u, AUX_SC1);
      __builtin_amdgcn_raw_buffer_store_b128(v4u{__float_as_uint(f0), tag, __float_as_uint(f1), tag}, rws, pb < 28 ? lane * 16 : OOR,
                                             WS_P + (unsigned)((hw * 60 + 32 + (pb < 28 ? pb : 0)) * 8 + kq) * 1024u, AUX_SC1);
    }
    {
      const unsigned qb = WS_P + (unsigned)((b * 60 + h) * 8 + 2 * hw) * 1024u;
      wait_until<4>(tb + QDELAY);
      if (hw == 1) {
        const v2u t0 = __builtin_amdgcn_raw_buffer_load_b64(rws, (COMMON_ORG && lane < 8) ? WS_T0 + (unsigned)idx * 128u + (unsigned)lane * 8u : OOR, 0, AUX_SC1);
        float d = (lane < 8 && t0[1] == tag) ? (float)(int)(tb - t0[0]) : 0.f;
        d = fmaxf(d, dpp<XOR1>(d)); d = fmaxf(d, dpp<XOR2>(d)); d = fmaxf(d, dpp<XOR7>(d));
        if (lane == 0) lds[L_TAG + 1] = tb - (unsigned)min((int)d, 400);
      }
      v4u pa, pc;
#pragma unroll 1
      for (unsigned spin = 0;; ++spin) {
        MEM_FENCE();
        pa = ld16<AUX_SC1>(rws, lane * 16, qb);
        pc = ld16<AUX_SC1>(rws, lane * 16 + 1024, qb);
        if (__all(pa[1] == tag && pa[3] == tag && pc[1] == tag && pc[3] == tag) || spin > SPIN_CAP) break;
        __builtin_amdgcn_s_sleep(8);
      }
      *reinterpret_cast<f32x2*>(ldsf + L_QP + hw * 128 + 2 * lane) =
          f32x2{__uint_as_float(pa[0]) + __uint_as_float(pc[0]), __uint_as_float(pa[2]) + __uint_as_float(pc[2])};
    }
    STAMP(2);
    wg_barrier();
    unsigned hp;
    {
      const int seg = lane / 14, piece = lane - seg * 14;
      const int col = 28 * u + 2 * piece, kk = 2 * hw + (seg & 1), mat = seg >> 1;
      const unsigned o = lane < 56 ? WS_P + (unsigned)(((b * 60 + 32 + 14 * mat + (col >> 7)) * 8 + kk) * 128 + (col & 127)) * 8u : OOR;
      v4u gu;
#pragma unroll 1
      for (unsigned spin = 0;; ++spin) {
        MEM_FENCE();
        gu = ld16<AUX_SC1>(rws, o, 0);
        if (__all(lane >= 56 || (gu[1] == tag && gu[3] == tag)) || spin > SPIN_CAP || ABL_NOW) break;
        __builtin_amdgcn_s_sleep(64);
      }
      if (lane < 56) *reinterpret_cast<v2u*>(lds + L_GU + (kk * 2 + mat) * 32 + 2 * piece) = v2u{gu[0], gu[2]};
      lds_signal(lds + L_FL + 8 + hw, lane);
      lds_wait4(lds + L_FL + 8);
      const int j = 8 * r + (c & 7);
      float gg = 0.f, uu = 0.f;
#pragma unroll
      for (int k8 = 0; k8 < 8; ++k8) { gg += ldsf[L_GU + k8 * 64 + j]; uu += ldsf[L_GU + k8 * 64 + 32 + j]; }
      gg = rbf(gg); uu = rbf(uu);
      const float sg = rbf(gg / (1.f + __expf(-gg)));
      const float hv = j < 28 ? sg * uu : 0.f;
      hp = pk_bf16(hv, dpp<XOR1>(hv));
      asm volatile("" : "+v"(hp));
    }
    const unsigned wphase = (unsigned)hh * WIN_PHASE;
    const unsigned cyc0 = ((((tb + WIN_START + wphase) >> (WIN_SHIFT + 1)) + 1u) << (WIN_SHIFT + 1)) - wphase;
    float y[8];
#pragma unroll
    for (int k = 0; k < 8; ++k) y[k] = 0.f;
    {
      const rsrc_t rd = mk_rsrc(wd + (size_t)(28 * u) * DM + 128 * hw, ABL_NOW ? 0 : 27 * 1024 + 256);
#define DSTEP(J)                                                                                 \
      {                                                                                          \
        wait_until<2>(cyc0 + ((unsigned)(J) << WIN_SHIFT));                                      \
        const v4u d0 = ld16<AUX_NT>(rd, r * (8 * 1024) + c * 16, (2 * (J)) * 1024);              \
        const v4u d1 = ld16<AUX_NT>(rd, r * (8 * 1024) + c * 16, (2 * (J) + 1) * 1024);          \
        gemv_pair(y, d0, d1, dppu<BCAST + 2 * (J)>(hp));                                         \
        PIN8(y);                                                                                 \
      }
      DSTEP(0) DSTEP(1) DSTEP(2) DSTEP(3)
#undef DSTEP
      const rsrc_t ro = mk_rsrc(wo + (size_t)(h * DH) * DM + 128 * hw, ABL_NOW ? 0 : 127 * 1024 + 256);
#pragma unroll
      for (int j = 0; j < 16; ++j) {
        wait_until<2>(cyc0 + ((unsigned)(4 + j) << WIN_SHIFT));
        const v4u w0 = ld16<AUX_NT>(ro, r * (32 * 1024) + c * 16, (2 * j) * 1024);
        const v4u w1 = ld16<AUX_NT>(ro, r * (32 * 1024) + c * 16, (2 * j + 1) * 1024);
#pragma unroll
        for (int k = 0; k < 4; ++k) { W[2 * j][k] = perm_lo(w0[k], w1[k]); W[2 * j + 1][k] = perm_hi(w0[k], w1[k]); }
        asm volatile("" : "+v"(W[2 * j]), "+v"(W[2 * j + 1]) :: "memory");
        if (j == 8) {
#pragma unroll 1
          for (unsigned spin = 0; spin < SPIN_CAP; ++spin) {
            if (*(lds_vu32*)(lds + L_FL + 16 + hw) != 0u) break;
            __builtin_amdgcn_s_sleep(16);
          }
          MEM_FENCE();
          const float mw = ldsf[L_MX + hw];
          float l = 0.f;
#pragma unroll
          for (int g = 0; g < 16; ++g) l += __builtin_amdgcn_exp2f((ldsf[L_SC + hw * 1024 + g * 64 + lane] - mw) * LOG2E);
          l = wave_sum(l);
          __builtin_amdgcn_raw_buffer_store_b128(v4u{__float_as_uint(mw), tag, __float_as_uint(l), tag}, rws,
                                                 lane == 0 ? WS_ML + (unsigned)((bh * 2 + s) * 4 + hw) * 16u : OOR, 0, AUX_SC1);
        }
      }
    }
    STAMP(3);
    float f[4];
    {
#pragma unroll 1
      for (unsigned spin = 0; spin < SPIN_CAP; ++spin) {
        if (*(lds_vu32*)(lds + L_FL + 12 + hw) != 0u) break;
        __builtin_amdgcn_s_sleep(32);
      }
      const unsigned o = lane < 8 ? WS_ML + (unsigned)(bh * 8 + lane) * 16u : OOR;
      v4u ml;
#pragma unroll 1
      for (unsigned spin = 0;; ++spin) {
        MEM_FENCE();
        ml = ld16<AUX_SC1>(rws, o, 0);
        if (__all(lane >= 8 || (ml[1] == tag && ml[3] == tag)) || spin > SPIN_CAP) break;
        __builtin_amdgcn_s_sleep(64);
      }
      const float mw = lane < 8 ? __uint_as_float(ml[0]) : -INFINITY;
      float M = fmaxf(mw, dpp<XOR1>(mw)); M = fmaxf(M, dpp<XOR2>(M)); M = fmaxf(M, dpp<XOR7>(M));
      const float wt = lane < 8 ? __builtin_amdgcn_exp2f((mw - M) * LOG2E) : 0.f;
      float L = wt * (lane < 8 ? __uint_as_float(ml[2]) : 0.f);
      L += dpp<XOR1>(L); L += dpp<XOR2>(L); L += dpp<XOR7>(L);
      const float fw = wt / L;
#pragma unroll
      for (int w = 0; w < 4; ++w) f[w] = __uint_as_float(__builtin_amdgcn_readlane(__float_as_uint(fw), 4 * s + w));
      PIN8(y);
    }
    STAMP(4);
    wg_barrier();
    STAMP(5);
    {
      float o0 = 0.f, o1 = 0.f;
#pragma unroll
      for (int w = 0; w < 4; ++w) {
        const f32x2 t = *reinterpret_cast<const f32x2*>(ldsf + L_OP + w * 128 + 32 * r + 2 * c);
        o0 = fmaf(f[w], t[0], o0); o1 = fmaf(f[w], t[1], o1);
      }
      const unsigned opk = pk_bf16(o0, o1);
#define YGEMV(J)                                                                                 \
  {                                                                                              \
    const unsigned ob = dppu<BCAST + (J)>(opk);                                                  \
    _Pragma("unroll") for (int k = 0; k < 4; ++k) {                                              \
      y[2 * k] = dot2(W[2 * (J)][k], ob, y[2 * k]);                                              \
      y[2 * k + 1] = dot2(W[2 * (J) + 1][k], ob, y[2 * k + 1]);                                  \
    }                                                                                            \
  }
      YGEMV(0) YGEMV(1) YGEMV(2) YGEMV(3) YGEMV(4) YGEMV(5) YGEMV(6) YGEMV(7)
      YGEMV(8) YGEMV(9) YGEMV(10) YGEMV(11) YGEMV(12) YGEMV(13) YGEMV(14) YGEMV(15)
#undef YGEMV
      float y0, y1;
      rows_reduce8(y, y0, y1);
      const unsigned o = WS_Y + (unsigned)(b * 64 + 16 * hw + c) * 4096u + (unsigned)u * 64u + (unsigned)r * 16u;
      __builtin_amdgcn_raw_buffer_store_b128(v4u{__float_as_uint(y0), tag, __float_as_uint(y1), tag}, rws, o, 0, AUX_SC1);
      STAMP(6);
    }
  }
}

}

extern "C" void kernel_launch(void* const* d_in, const int* in_sizes, int n_in, void* d_out, int out_size, void* d_ws, size_t ws_size,
                              hipStream_t stream) {
  (void)in_sizes; (void)n_in; (void)out_size; (void)ws_size;
  hipLaunchKernelGGL(fused_decode_kernel, dim3(256), dim3(512), 0, stream, (const u16*)d_in[0], (const u16*)d_in[1], (const u16*)d_in[2],
                     (const u16*)d_in[3], (const u16*)d_in[4], (const u16*)d_in[6], (const u16*)d_in[7], (const u16*)d_in[8], (u16*)d_out,
                     (unsigned char*)d_ws);
}
```

```cpp
#define TAU_NS 2285
#define SA_NS 600
#include <hip/hip_runtime.h>
#include <hip/hip_bf16.h>
#include <stdint.h>

typedef unsigned short u16;
typedef unsigned v4u __attribute__((ext_vector_type(4)));
typedef unsigned v2u __attribute__((ext_vector_type(2)));
typedef float f32x4 __attribute__((ext_vector_type(4)));
typedef float f32x2 __attribute__((ext_vector_type(2)));
typedef __bf16 bf16x2 __attribute__((ext_vector_type(2)));
typedef __amdgpu_buffer_rsrc_t rsrc_t;
typedef __attribute__((address_space(3))) volatile unsigned lds_vu32;
typedef __attribute__((address_space(3))) volatile v4u lds_vv4u;

namespace {

constexpr int DM = 512, NH = 32, SK = 8192, DH = 128, FF = 1792;
constexpr float SCALE = 0.08838834764831845f;
constexpr float LOG2E = 1.4426950408889634f;
constexpr int AUX_NT = 2;
constexpr int AUX_SC1 = 16;
constexpr unsigned TAG_STEP = 0x9E3779B9u;
constexpr unsigned SPIN_CAP = 1u << 18;
constexpr unsigned OOR = 0x7ffffff0u;

#define DIST 2
#define SB_NS 100
constexpr int TAU256 = TAU_NS * 256 / 40;
#define QDELAY 450
#define LAY 1
#define COMMON_ORG 1
#define YDELAY 115
#define YSLEEP 4
#define WIN_SHIFT 8
#define WIN_PHASE 64
#define WIN_START 1000
#define AUX_K 2
#define AUX_V 2
#define ABL_NOW 0

constexpr unsigned WS_EPOCH = 0;
constexpr unsigned WS_T0 = 8192;
constexpr unsigned WS_ML = 16384;
constexpr unsigned WS_P = WS_ML + 128 * 128;
constexpr unsigned WS_Y = WS_P + 4 * 60 * 8 * 1024;
constexpr unsigned WS_END = WS_Y + 4 * 64 * 64 * 64;

constexpr int L_SC = 0;
constexpr int L_XN = L_SC + 4096;
constexpr int L_QP = L_XN + 1024;
constexpr int L_OP = L_QP + 512;
constexpr int L_GU = L_OP + 512;
constexpr int L_FL = L_GU + 512;
constexpr int L_MX = L_FL + 20;
constexpr int L_TAG = L_MX + 4;
constexpr int L_END = L_TAG + 4;

__device__ __forceinline__ rsrc_t mk_rsrc(const void* p, unsigned bytes) {
  return __builtin_amdgcn_make_buffer_rsrc(const_cast<void*>(p), 0, bytes, 0x00020000);
}
template <int AUX> __device__ __forceinline__ v4u ld16(rsrc_t r, unsigned voff, unsigned soff) {
  return __builtin_amdgcn_raw_buffer_load_b128(r, voff, soff, AUX);
}
__device__ __forceinline__ float dot2(unsigned a, unsigned b, float acc) {
  return __builtin_amdgcn_fdot2_f32_bf16(__builtin_bit_cast(bf16x2, a), __builtin_bit_cast(bf16x2, b), acc, false);
}
__device__ __forceinline__ float bflo(unsigned w) { return __uint_as_float(w << 16); }
__device__ __forceinline__ float bfhi(unsigned w) { return __uint_as_float(w & 0xffff0000u); }
__device__ __forceinline__ unsigned pk_bf16(float lo, float hi) {
  bf16x2 v = {(__bf16)lo, (__bf16)hi};
  return __builtin_bit_cast(unsigned, v);
}
__device__ __forceinline__ float rbf(float x) { return (float)(__bf16)x; }
template <int CTRL> __device__ __forceinline__ float dpp(float x) {
  return __builtin_bit_cast(float, __builtin_amdgcn_mov_dpp(__builtin_bit_cast(int, x), CTRL, 0xf, 0xf, true));
}
template <int CTRL> __device__ __forceinline__ unsigned dppu(unsigned x) {
  return (unsigned)__builtin_amdgcn_mov_dpp((int)x, CTRL, 0xf, 0xf, true);
}
constexpr int XOR1 = 0xB1, XOR2 = 0x4E, XOR7 = 0x141, XOR8 = 0x128, ROR4 = 0x124, BCAST = 0x150;
__device__ __forceinline__ float swap32_add(float a, float b) {
  auto t = __builtin_amdgcn_permlane32_swap(__float_as_uint(a), __float_as_uint(b), false, false);
  return __uint_as_float(t[0]) + __uint_as_float(t[1]);
}
__device__ __forceinline__ float swap16_add(float a, float b) {
  auto t = __builtin_amdgcn_permlane16_swap(__float_as_uint(a), __float_as_uint(b), false, false);
  return __uint_as_float(t[0]) + __uint_as_float(t[1]);
}
__device__ __forceinline__ float wave_sum(float x) {
  x += dpp<XOR1>(x); x += dpp<XOR2>(x); x += dpp<XOR7>(x); x += dpp<XOR8>(x);
  x = swap16_add(x, x); x = swap32_add(x, x);
  return x;
}
__device__ __forceinline__ float wave_max(float x) {
  x = fmaxf(x, dpp<XOR1>(x)); x = fmaxf(x, dpp<XOR2>(x)); x = fmaxf(x, dpp<XOR7>(x)); x = fmaxf(x, dpp<XOR8>(x));
  auto s = __builtin_amdgcn_permlane16_swap(__float_as_uint(x), __float_as_uint(x), false, false);
  x = fmaxf(__uint_as_float(s[0]), __uint_as_float(s[1]));
  auto t = __builtin_amdgcn_permlane32_swap(__float_as_uint(x), __float_as_uint(x), false, false);
  return fmaxf(__uint_as_float(t[0]), __uint_as_float(t[1]));
}
__device__ __forceinline__ void rows_reduce8(const float (&a)[8], float& o0, float& o1) {
  const float t0 = swap32_add(a[0], a[4]), t1 = swap32_add(a[1], a[5]), t2 = swap32_add(a[2], a[6]), t3 = swap32_add(a[3], a[7]);
  o0 = swap16_add(t0, t2); o1 = swap16_add(t1, t3);
}
__device__ __forceinline__ void wg_barrier() { asm volatile("s_waitcnt lgkmcnt(0)\n\ts_barrier" ::: "memory"); }
#define SCHED_FENCE() __builtin_amdgcn_sched_barrier(0)
#define MEM_FENCE() asm volatile("" ::: "memory")
#define PIN4(a) asm volatile("" : "+v"((a)[0]), "+v"((a)[1]), "+v"((a)[2]), "+v"((a)[3]) :: "memory")
#define PIN8(a) asm volatile("" : "+v"((a)[0]), "+v"((a)[1]), "+v"((a)[2]), "+v"((a)[3]), "+v"((a)[4]), "+v"((a)[5]), "+v"((a)[6]), "+v"((a)[7]) :: "memory")
__device__ __forceinline__ unsigned perm_lo(unsigned a, unsigned b) { return __builtin_amdgcn_perm(b, a, 0x05040100u); }
__device__ __forceinline__ unsigned perm_hi(unsigned a, unsigned b) { return __builtin_amdgcn_perm(b, a, 0x07060302u); }
__device__ __forceinline__ void gemv_pair(float (&acc)[8], const v4u& ra, const v4u& rb, unsigned xp) {
#pragma unroll
  for (int k = 0; k < 4; ++k) {
    acc[2 * k] = dot2(perm_lo(ra[k], rb[k]), xp, acc[2 * k]);
    acc[2 * k + 1] = dot2(perm_hi(ra[k], rb[k]), xp, acc[2 * k + 1]);
  }
}
__device__ __forceinline__ void lds_signal(unsigned* fl, int lane) {
  asm volatile("s_waitcnt lgkmcnt(0)" ::: "memory");
  if (lane == 0) *(lds_vu32*)fl = 1u;
}
__device__ __forceinline__ void lds_wait4(unsigned* fl4) {
#pragma unroll 1
  for (unsigned spin = 0; spin < (SPIN_CAP << 4); ++spin) {
    const v4u f = *(lds_vv4u*)fl4;
    if ((f[0] & f[1] & f[2] & f[3]) != 0u) break;
  }
  MEM_FENCE();
}
__device__ __forceinline__ unsigned now32() { return (unsigned)__builtin_amdgcn_s_memrealtime(); }
template <int SLEEP> __device__ __forceinline__ void wait_until(unsigned t) {
#pragma unroll 1
  for (int sp = 0; sp < 4096; ++sp) {
    if ((int)(now32() - t) >= 0) break;
    __builtin_amdgcn_s_sleep(SLEEP);
  }
  MEM_FENCE();
  SCHED_FENCE();
}
__device__ __forceinline__ void proj_part(const v4u* W, const v2u (&xp)[4], float* dst) {
  unsigned wp[16];
#pragma unroll
  for (int k = 0; k < 4; ++k) {
    wp[2 * k] = perm_lo(W[0][k], W[1][k]); wp[2 * k + 1] = perm_hi(W[0][k], W[1][k]);
    wp[8 + 2 * k] = perm_lo(W[2][k], W[3][k]); wp[8 + 2 * k + 1] = perm_hi(W[2][k], W[3][k]);
  }
  float t[8];
#pragma unroll
  for (int k = 0; k < 8; ++k) {
    const float a0 = dot2(wp[8 + k], xp[0][1], dot2(wp[k], xp[0][0], 0.f)), a1 = dot2(wp[8 + k], xp[1][1], dot2(wp[k], xp[1][0], 0.f));
    const float a2 = dot2(wp[8 + k], xp[2][1], dot2(wp[k], xp[2][0], 0.f)), a3 = dot2(wp[8 + k], xp[3][1], dot2(wp[k], xp[3][0], 0.f));
    t[k] = swap16_add(swap32_add(a0, a2), swap32_add(a1, a3));
  }
  *reinterpret_cast<f32x4*>(dst) = f32x4{t[0], t[1], t[2], t[3]};
  *reinterpret_cast<f32x4*>(dst + 4) = f32x4{t[4], t[5], t[6], t[7]};
}

template <bool VPASS = false>
__device__ __forceinline__ void ld_quarter(v4u* R, rsrc_t rk, rsrc_t rv, unsigned voff, int n, int rot, unsigned tb) {
  if (TAU256 > 0) {
    const unsigned due = tb + (unsigned)(SA_NS / 10) + (unsigned)((n * TAU256) >> 8) + (unsigned)((n < 12 ? n : 12) * (SB_NS / 10));
#pragma unroll 1
    for (int sp = 0; sp < 512; ++sp) {
      if ((int)(now32() - due) >= 0 || n >= 128) break;
      __builtin_amdgcn_s_sleep(1);
    }
    SCHED_FENCE();
  }
  const rsrc_t rn = n < 64 ? rk : rv;
  const unsigned nr = (unsigned)(n + rot) & 63u;
  const unsigned off = n < 128 ? (LAY == 1 ? nr << 14 : ((nr >> 1) << 15) + ((nr & 1u) << 12)) : (1u << 20);
  if (AUX_K == AUX_V) {
#pragma unroll
    for (int i = 0; i < 4; ++i) R[i] = ld16<AUX_K>(rn, voff + i * 1024, off);
  } else if (VPASS) {
#pragma unroll
    for (int i = 0; i < 4; ++i) R[i] = ld16<AUX_V>(rn, voff + i * 1024, off);
  } else {
#pragma unroll
    for (int i = 0; i < 4; ++i) R[i] = ld16<AUX_K>(rn, voff + i * 1024, off);
  }
  SCHED_FENCE();
}
#define WAIT_Q() asm volatile("s_waitcnt vmcnt(4)" ::: "memory")
#define WAIT_PRO() asm volatile("s_waitcnt vmcnt(4)" ::: "memory")

template <bool REFILL, int SKIP = 0>
__device__ __forceinline__ float k_group(v4u* R, const v4u& q, int c, rsrc_t rk, rsrc_t rv, unsigned voff, int n0, int rot, unsigned tb) {
  float x[16];
#pragma unroll
  for (int k = 0; k < 4; ++k) {
    if (REFILL && k >= SKIP) {
      WAIT_Q();
      ld_quarter(R + 4 * ((k + DIST) & 3), rk, rv, voff, n0 + k + DIST, rot, tb);
    }
#pragma unroll
    for (int i = 4 * k; i < 4 * k + 4; ++i) {
      float d = dot2(R[i][0], q[0], 0.f); d = dot2(R[i][1], q[1], d); d = dot2(R[i][2], q[2], d); x[i] = dot2(R[i][3], q[3], d);
    }
    PIN4(x + 4 * k);
    SCHED_FENCE();
  }
#define TR_STEP(HALF, CTRL, BIT)                                                                 \
  _Pragma("unroll") for (int hh_ = 0; hh_ < (HALF); ++hh_) {                                     \
    const float lo_ = x[hh_], hi_ = x[hh_ + (HALF)];                                             \
    const float t1_ = lo_ + dpp<CTRL>(lo_), t2_ = hi_ + dpp<CTRL>(hi_);                          \
    x[hh_] = (c & (BIT)) ? t2_ : t1_;                                                            \
  }
  TR_STEP(8, XOR8, 8) TR_STEP(4, XOR7, 4) TR_STEP(2, XOR2, 2) TR_STEP(1, XOR1, 1)
#undef TR_STEP
  return rbf(rbf(x[0]) * SCALE);
}

template <int I> __device__ __forceinline__ void pv_one(float (&acc)[8], const v4u& V, float p) {
  const float pb = dpp<BCAST + I>(p);
#pragma unroll
  for (int k = 0; k < 4; ++k) {
    acc[2 * k] = fmaf(pb, bflo(V[k]), acc[2 * k]);
    acc[2 * k + 1] = fmaf(pb, bfhi(V[k]), acc[2 * k + 1]);
  }
}

#define STAMP(i) do { } while (0)

__global__ void __launch_bounds__(512, 2)
fused_decode_kernel(const u16* __restrict__ x, const u16* __restrict__ kc, const u16* __restrict__ vc, const u16* __restrict__ wq,
                    const u16* __restrict__ wo, const u16* __restrict__ wg, const u16* __restrict__ wu, const u16* __restrict__ wd,
                    u16* __restrict__ out, unsigned char* ws) {
  __shared__ __attribute__((aligned(16))) unsigned lds[L_END];
  float* ldsf = reinterpret_cast<float*>(lds);
  const int tid = threadIdx.x, lane = tid & 63;
  const int wave = __builtin_amdgcn_readfirstlane(tid >> 6) ^ 4;
  const int r = lane >> 4, c = lane & 15;
  const int bid = blockIdx.x, xg = bid & 7, idx = bid >> 3;
  const int hh = idx >> 3, h = xg + 8 * hh, b = (idx >> 1) & 3, s = idx & 1;
  const int bh = b * NH + h, u = h * 2 + s;
  const rsrc_t rws = mk_rsrc(ws, WS_END);
  const unsigned tb = now32();
  STAMP(0);

  if (wave < 4) {
    const size_t kvoff = ((size_t)bh * SK + (size_t)s * (SK / 2)) * DH;
    const rsrc_t rk = mk_rsrc(kc + kvoff, 1u << 20), rv = mk_rsrc(vc + kvoff, 1u << 20);
    const unsigned voff = lane * 16 + wave * (LAY == 1 ? 4096 : 8192);
    const int rot = bh * 2 + s;
    const unsigned ph = (unsigned)((((idx + xg) & 7) * TAU256) >> 11);
    const unsigned ta = tb + ph;
    v4u P[32], R[16];
    wg_barrier();
    ld_quarter(P, rk, rv, voff, 0, rot, ta);
    ld_quarter(P + 4, rk, rv, voff, 1, rot, ta);
#pragma unroll
    for (int k = 2; k < 8; ++k) { WAIT_PRO(); ld_quarter(P + 4 * k, rk, rv, voff, k, rot, ta); }
#pragma unroll
    for (int k = 0; k < 4; ++k) { WAIT_PRO(); ld_quarter(R + 4 * k, rk, rv, voff, 8 + k, rot, ta); }
    wg_barrier();
    STAMP(1);
    v4u q;
    {
      float qs[8];
#pragma unroll
      for (int k = 0; k < 8; ++k) qs[k] = 0.f;
#pragma unroll
      for (int w = 0; w < 4; ++w) {
        const f32x4 a0 = *reinterpret_cast<const f32x4*>(ldsf + L_QP + w * 128 + 8 * c);
        const f32x4 a1 = *reinterpret_cast<const f32x4*>(ldsf + L_QP + w * 128 + 8 * c + 4);
#pragma unroll
        for (int k = 0; k < 4; ++k) { qs[k] += a0[k]; qs[4 + k] += a1[k]; }
      }
#pragma unroll
      for (int k = 0; k < 4; ++k) q[k] = pk_bf16(qs[2 * k], qs[2 * k + 1]);
    }
    const unsigned tag = lds[L_TAG];
    const unsigned tc = __builtin_amdgcn_readfirstlane(lds[L_TAG + 1]) + ph;
    float* sc = ldsf + L_SC + wave * 1024 + lane;
    float mx = k_group<true, 4 - DIST>(R, q, c, rk, rv, voff, 8, rot, tc);
    sc[2 * 64] = mx;
    {
      const float sv = k_group<false>(P, q, c, rk, rv, voff, 0, rot, tc);
      sc[0] = sv;
      mx = fmaxf(mx, sv);
    }
#pragma unroll 1
    for (int g = 3; g < 16; ++g) {
      const float sv = k_group<true>(R, q, c, rk, rv, voff, 4 * g, rot, tc);
      sc[g * 64] = sv;
      mx = fmaxf(mx, sv);
      if (g == 3) {
        const float sw = k_group<false>(P + 16, q, c, rk, rv, voff, 0, rot, tc);
        sc[64] = sw;
        mx = fmaxf(mx, sw);
      }
    }
    STAMP(2);
    mx = wave_max(mx);
    if (lane == 0) ldsf[L_MX + wave] = mx;
    lds_signal(lds + L_FL + 16 + wave, lane);
    STAMP(3);
    float acc[8];
#pragma unroll
    for (int k = 0; k < 8; ++k) acc[k] = 0.f;
#pragma unroll 1
    for (int g = 0; g < 16; ++g) {
      const float p = __builtin_amdgcn_exp2f((sc[g * 64] - mx) * LOG2E);
      if (g == 10 && lane == 0) *(lds_vu32*)(lds + L_FL + 12 + wave) = 1u;
      const int n0 = 64 + 4 * g + DIST;
#define PV_Q(k)                                                                                  \
      WAIT_Q();                                                                                  \
      ld_quarter<true>(R + 4 * (((k) + DIST) & 3), rv, rv, voff, n0 + (k), rot, tc);             \
      pv_one<4 * (k)>(acc, R[4 * (k)], p); pv_one<4 * (k) + 1>(acc, R[4 * (k) + 1], p);          \
      pv_one<4 * (k) + 2>(acc, R[4 * (k) + 2], p); pv_one<4 * (k) + 3>(acc, R[4 * (k) + 3], p);  \
      PIN8(acc);                                                                                 \
      SCHED_FENCE();
      PV_Q(0) PV_Q(1) PV_Q(2) PV_Q(3)
#undef PV_Q
    }
    {
      float o0, o1;
      rows_reduce8(acc, o0, o1);
      *reinterpret_cast<f32x2*>(ldsf + L_OP + wave * 128 + 8 * c + 2 * r) = f32x2{o0, o1};
    }
    STAMP(4);
    wg_barrier();
    STAMP(5);
    if (wave != 0) return;
    const unsigned xw = *reinterpret_cast<const unsigned*>(x + b * DM + 8 * u + 2 * (lane & 3));
    const unsigned ybase = WS_Y + (unsigned)(b * 64 + u) * 4096u;
    wait_until<1>(now32() + YDELAY);
    v4u g0, g1, g2, g3;
#pragma unroll 1
    for (unsigned spin = 0;; ++spin) {
      MEM_FENCE();
      g0 = ld16<AUX_SC1>(rws, lane * 16, ybase);
      g1 = ld16<AUX_SC1>(rws, lane * 16 + 1024, ybase);
      g2 = ld16<AUX_SC1>(rws, lane * 16 + 2048, ybase);
      g3 = ld16<AUX_SC1>(rws, lane * 16 + 3072, ybase);
      const bool ok = g0[1] == tag && g0[3] == tag && g1[1] == tag && g1[3] == tag && g2[1] == tag && g2[3] == tag && g3[1] == tag && g3[3] == tag;
      if (__all(ok) || spin > SPIN_CAP) break;
      __builtin_amdgcn_s_sleep(YSLEEP);
    }
    float y0 = (__uint_as_float(g0[0]) + __uint_as_float(g1[0])) + (__uint_as_float(g2[0]) + __uint_as_float(g3[0]));
    float y1 = (__uint_as_float(g0[2]) + __uint_as_float(g1[2])) + (__uint_as_float(g2[2]) + __uint_as_float(g3[2]));
    y0 += dpp<ROR4>(y0); y1 += dpp<ROR4>(y1);
    y0 += dpp<XOR8>(y0); y1 += dpp<XOR8>(y1);
    y0 = swap16_add(y0, y0); y1 = swap16_add(y1, y1);
    y0 = swap32_add(y0, y0); y1 = swap32_add(y1, y1);
    if (lane < 4) *reinterpret_cast<unsigned*>(out + b * DM + 8 * u + 2 * lane) = pk_bf16(bflo(xw) + y0, bfhi(xw) + y1);
    STAMP(6);
    if (u == 0 && b == 0 && lane == 0) __builtin_amdgcn_raw_buffer_store_b32(tag, rws, WS_EPOCH, 0, AUX_SC1);
  } else {
    const int hw = wave - 4;
    const v4u xr = ld16<0>(mk_rsrc(x + hw * DM, DM * 2), lane * 16, 0);
    const unsigned epoch = __builtin_amdgcn_raw_buffer_load_b32(rws, 0, WS_EPOCH, AUX_SC1);
    SCHED_FENCE();
    const int cq = xg * 32 + idx, pb = cq >> 3, kq = cq & 7;
    v4u W[32];
    {
      const rsrc_t rq = mk_rsrc(wq + (size_t)(64 * kq + 16 * hw) * (NH * DH) + pb * DH, 15 * 8192 + 256);
#pragma unroll
      for (int i = 0; i < 4; ++i) W[i] = ld16<AUX_NT>(rq, r * (4 * 8192) + c * 16, i * 8192);
      const u16* wf = pb < 14 ? wg + pb * 128 : wu + (pb < 28 ? pb - 14 : 0) * 128;
      const rsrc_t rf = mk_rsrc(wf + (size_t)(64 * kq + 16 * hw) * FF, (pb < 28 && !ABL_NOW) ? 15 * FF * 2 + 256 : 0);
#pragma unroll
      for (int i = 0; i < 4; ++i) W[4 + i] = ld16<AUX_NT>(rf, r * (4 * FF * 2) + c * 16, i * (FF * 2));
    }
    SCHED_FENCE();
    if (hw == 0 && lane < 20) lds[L_FL + lane] = 0u;
    wg_barrier();
    {
      float xv[8];
#pragma unroll
      for (int k = 0; k < 4; ++k) { xv[2 * k] = bflo(xr[k]); xv[2 * k + 1] = bfhi(xr[k]); }
      const float sm = ((xv[0] + xv[1]) + (xv[2] + xv[3])) + ((xv[4] + xv[5]) + (xv[6] + xv[7]));
      const float mean = wave_sum(sm) * (1.f / DM);
      float sq = 0.f;
#pragma unroll
      for (int k = 0; k < 8; ++k) { const float d = xv[k] - mean; sq = fmaf(d, d, sq); }
      const float var = wave_sum(sq) * (1.f / DM);
      const float mb = rbf(mean);
      const float sd = rbf(sqrtf(rbf(rbf(var) + 1e-5f)));
      const float ri = 1.f / sd;
      float xn[8];
#pragma unroll
      for (int k = 0; k < 8; ++k) {
        const float d = rbf(xv[k] - mb);
        const float q0 = d * ri;
        xn[k] = fmaf(fmaf(-q0, sd, d), ri, q0);
      }
      *reinterpret_cast<v4u*>(lds + L_XN + hw * 256 + 4 * lane) =
          v4u{pk_bf16(xn[0], xn[1]), pk_bf16(xn[2], xn[3]), pk_bf16(xn[4], xn[5]), pk_bf16(xn[6], xn[7])};
    }
    const unsigned tag = epoch + TAG_STEP;
    if (hw == 0 && lane == 0) lds[L_TAG] = tag;
    if (COMMON_ORG && hw == 1 && idx == 0)
      __builtin_amdgcn_raw_buffer_store_b64(v2u{tb, tag}, rws, lane < 32 ? WS_T0 + (unsigned)lane * 128u + (unsigned)xg * 8u : OOR, 0, AUX_SC1);
    lds_signal(lds + L_FL + hw, lane);
    STAMP(1);
    lds_wait4(lds + L_FL);
    {
      v2u xp[4];
#pragma unroll
      for (int bb = 0; bb < 4; ++bb) xp[bb] = *reinterpret_cast<const v2u*>(lds + L_XN + bb * 256 + 32 * kq + 8 * hw + 2 * r);
      proj_part(W, xp, ldsf + L_SC + (hw * 4 + r) * 128 + 8 * c);
      proj_part(W + 4, xp, ldsf + L_SC + 2048 + (hw * 4 + r) * 128 + 8 * c);
      lds_signal(lds + L_FL + 4 + hw, lane);
      lds_wait4(lds + L_FL + 4);
      float q0 = 0.f, q1 = 0.f, f0 = 0.f, f1 = 0.f;
#pragma unroll
      for (int w = 0; w < 4; ++w) {
        const f32x2 tq = *reinterpret_cast<const f32x2*>(ldsf + L_SC + (w * 4 + hw) * 128 + 2 * lane);
        const f32x2 tf = *reinterpret_cast<const f32x2*>(ldsf + L_SC + 2048 + (w * 4 + hw) * 128 + 2 * lane);
        q0 += tq[0]; q1 += tq[1]; f0 += tf[0]; f1 += tf[1];
      }
      __builtin_amdgcn_raw_buffer_store_b128(v4u{__float_as_uint(q0), tag, __float_as_uint(q1), tag}, rws, lane * 16,
                                             WS_P + (unsigned)((hw * 60 + pb) * 8 + kq) * 1024u, AUX_SC1);
      __builtin_amdgcn_raw_buffer_store_b128(v4u{__float_as_uint(f0), tag, __float_as_uint(f1), tag}, rws, pb < 28 ? lane * 16 : OOR,
                                             WS_P + (unsigned)((hw * 60 + 32 + (pb < 28 ? pb : 0)) * 8 + kq) * 1024u, AUX_SC1);
    }
    {
      const unsigned qb = WS_P + (unsigned)((b * 60 + h) * 8 + 2 * hw) * 1024u;
      wait_until<4>(tb + QDELAY);
      if (hw == 1) {
        const v2u t0 = __builtin_amdgcn_raw_buffer_load_b64(rws, (COMMON_ORG && lane < 8) ? WS_T0 + (unsigned)idx * 128u + (unsigned)lane * 8u : OOR, 0, AUX_SC1);
        float d = (lane < 8 && t0[1] == tag) ? (float)(int)(tb - t0[0]) : 0.f;
        d = fmaxf(d, dpp<XOR1>(d)); d = fmaxf(d, dpp<XOR2>(d)); d = fmaxf(d, dpp<XOR7>(d));
        if (lane == 0) lds[L_TAG + 1] = tb - (unsigned)min((int)d, 400);
      }
      v4u pa, pc;
#pragma unroll 1
      for (unsigned spin = 0;; ++spin) {
        MEM_FENCE();
        pa = ld16<AUX_SC1>(rws, lane * 16, qb);
        pc = ld16<AUX_SC1>(rws, lane * 16 + 1024, qb);
        if (__all(pa[1] == tag && pa[3] == tag && pc[1] == tag && pc[3] == tag) || spin > SPIN_CAP) break;
        __builtin_amdgcn_s_sleep(8);
      }
      *reinterpret_cast<f32x2*>(ldsf + L_QP + hw * 128 + 2 * lane) =
          f32x2{__uint_as_float(pa[0]) + __uint_as_float(pc[0]), __uint_as_float(pa[2]) + __uint_as_float(pc[2])};
    }
    STAMP(2);
    wg_barrier();
    unsigned hp;
    {
      const int seg = lane / 14, piece = lane - seg * 14;
      const int col = 28 * u + 2 * piece, kk = 2 * hw + (seg & 1), mat = seg >> 1;
      const unsigned o = lane < 56 ? WS_P + (unsigned)(((b * 60 + 32 + 14 * mat + (col >> 7)) * 8 + kk) * 128 + (col & 127)) * 8u : OOR;
      v4u gu;
#pragma unroll 1
      for (unsigned spin = 0;; ++spin) {
        MEM_FENCE();
        gu = ld16<AUX_SC1>(rws, o, 0);
        if (__all(lane >= 56 || (gu[1] == tag && gu[3] == tag)) || spin > SPIN_CAP || ABL_NOW) break;
        __builtin_amdgcn_s_sleep(64);
      }
      if (lane < 56) *reinterpret_cast<v2u*>(lds + L_GU + (kk * 2 + mat) * 32 + 2 * piece) = v2u{gu[0], gu[2]};
      lds_signal(lds + L_FL + 8 + hw, lane);
      lds_wait4(lds + L_FL + 8);
      const int j = 8 * r + (c & 7);
      float gg = 0.f, uu = 0.f;
#pragma unroll
      for (int k8 = 0; k8 < 8; ++k8) { gg += ldsf[L_GU + k8 * 64 + j]; uu += ldsf[L_GU + k8 * 64 + 32 + j]; }
      gg = rbf(gg); uu = rbf(uu);
      const float sg = rbf(gg / (1.f + __expf(-gg)));
      const float hv = j < 28 ? sg * uu : 0.f;
      hp = pk_bf16(hv, dpp<XOR1>(hv));
      asm volatile("" : "+v"(hp));
    }
    const unsigned wphase = (unsigned)hh * WIN_PHASE;
    const unsigned cyc0 = ((((tb + WIN_START + wphase) >> (WIN_SHIFT + 1)) + 1u) << (WIN_SHIFT + 1)) - wphase;
    float y[8];
#pragma unroll
    for (int k = 0; k < 8; ++k) y[k] = 0.f;
    {
      const rsrc_t rd = mk_rsrc(wd + (size_t)(28 * u) * DM + 128 * hw, ABL_NOW ? 0 : 27 * 1024 + 256);
#define DSTEP(J)                                                                                 \
      {                                                                                          \
        wait_until<2>(cyc0 + ((unsigned)(J) << WIN_SHIFT));                                      \
        const v4u d0 = ld16<AUX_NT>(rd, r * (8 * 1024) + c * 16, (2 * (J)) * 1024);              \
        const v4u d1 = ld16<AUX_NT>(rd, r * (8 * 1024) + c * 16, (2 * (J) + 1) * 1024);          \
        gemv_pair(y, d0, d1, dppu<BCAST + 2 * (J)>(hp));                                         \
        PIN8(y);                                                                                 \
      }
      DSTEP(0) DSTEP(1) DSTEP(2) DSTEP(3)
#undef DSTEP
      const rsrc_t ro = mk_rsrc(wo + (size_t)(h * DH) * DM + 128 * hw, ABL_NOW ? 0 : 127 * 1024 + 256);
#pragma unroll
      for (int j = 0; j < 16; ++j) {
        wait_until<2>(cyc0 + ((unsigned)(4 + j) << WIN_SHIFT));
        const v4u w0 = ld16<AUX_NT>(ro, r * (32 * 1024) + c * 16, (2 * j) * 1024);
        const v4u w1 = ld16<AUX_NT>(ro, r * (32 * 1024) + c * 16, (2 * j + 1) * 1024);
#pragma unroll
        for (int k = 0; k < 4; ++k) { W[2 * j][k] = perm_lo(w0[k], w1[k]); W[2 * j + 1][k] = perm_hi(w0[k], w1[k]); }
        asm volatile("" : "+v"(W[2 * j]), "+v"(W[2 * j + 1]) :: "memory");
        if (j == 8) {
#pragma unroll 1
          for (unsigned spin = 0; spin < SPIN_CAP; ++spin) {
            if (*(lds_vu32*)(lds + L_FL + 16 + hw) != 0u) break;
            __builtin_amdgcn_s_sleep(16);
          }
          MEM_FENCE();
          const float mw = ldsf[L_MX + hw];
          float l = 0.f;
#pragma unroll
          for (int g = 0; g < 16; ++g) l += __builtin_amdgcn_exp2f((ldsf[L_SC + hw * 1024 + g * 64 + lane] - mw) * LOG2E);
          l = wave_sum(l);
          __builtin_amdgcn_raw_buffer_store_b128(v4u{__float_as_uint(mw), tag, __float_as_uint(l), tag}, rws,
                                                 lane == 0 ? WS_ML + (unsigned)((bh * 2 + s) * 4 + hw) * 16u : OOR, 0, AUX_SC1);
        }
      }
    }
    STAMP(3);
    float f[4];
    {
#pragma unroll 1
      for (unsigned spin = 0; spin < SPIN_CAP; ++spin) {
        if (*(lds_vu32*)(lds + L_FL + 12 + hw) != 0u) break;
        __builtin_amdgcn_s_sleep(32);
      }
      const unsigned o = lane < 8 ? WS_ML + (unsigned)(bh * 8 + lane) * 16u : OOR;
      v4u ml;
#pragma unroll 1
      for (unsigned spin = 0;; ++spin) {
        MEM_FENCE();
        ml = ld16<AUX_SC1>(rws, o, 0);
        if (__all(lane >= 8 || (ml[1] == tag && ml[3] == tag)) || spin > SPIN_CAP) break;
        __builtin_amdgcn_s_sleep(64);
      }
      const float mw = lane < 8 ? __uint_as_float(ml[0]) : -INFINITY;
      float M = fmaxf(mw, dpp<XOR1>(mw)); M = fmaxf(M, dpp<XOR2>(M)); M = fmaxf(M, dpp<XOR7>(M));
      const float wt = lane < 8 ? __builtin_amdgcn_exp2f((mw - M) * LOG2E) : 0.f;
      float L = wt * (lane < 8 ? __uint_as_float(ml[2]) : 0.f);
      L += dpp<XOR1>(L); L += dpp<XOR2>(L); L += dpp<XOR7>(L);
      const float fw = wt / L;
#pragma unroll
      for (int w = 0; w < 4; ++w) f[w] = __uint_as_float(__builtin_amdgcn_readlane(__float_as_uint(fw), 4 * s + w));
      PIN8(y);
    }
    STAMP(4);
    wg_barrier();
    STAMP(5);
    {
      float o0 = 0.f, o1 = 0.f;
#pragma unroll
      for (int w = 0; w < 4; ++w) {
        const f32x2 t = *reinterpret_cast<const f32x2*>(ldsf + L_OP + w * 128 + 32 * r + 2 * c);
        o0 = fmaf(f[w], t[0], o0); o1 = fmaf(f[w], t[1], o1);
      }
      const unsigned opk = pk_bf16(o0, o1);
#define YGEMV(J)                                                                                 \
  {                                                                                              \
    const unsigned ob = dppu<BCAST + (J)>(opk);                                                  \
    _Pragma("unroll") for (int k = 0; k < 4; ++k) {                                              \
      y[2 * k] = dot2(W[2 * (J)][k], ob, y[2 * k]);                                              \
      y[2 * k + 1] = dot2(W[2 * (J) + 1][k], ob, y[2 * k + 1]);                                  \
    }                                                                                            \
  }
      YGEMV(0) YGEMV(1) YGEMV(2) YGEMV(3) YGEMV(4) YGEMV(5) YGEMV(6) YGEMV(7)
      YGEMV(8) YGEMV(9) YGEMV(10) YGEMV(11) YGEMV(12) YGEMV(13) YGEMV(14) YGEMV(15)
#undef YGEMV
      float y0, y1;
      rows_reduce8(y, y0, y1);
      const unsigned o = WS_Y + (unsigned)(b * 64 + 16 * hw + c) * 4096u + (unsigned)u * 64u + (unsigned)r * 16u;
      __builtin_amdgcn_raw_buffer_store_b128(v4u{__float_as_uint(y0), tag, __float_as_uint(y1), tag}, rws, o, 0, AUX_SC1);
      STAMP(6);
    }
  }
}

}

extern "C" void kernel_launch(void* const* d_in, const int* in_sizes, int n_in, void* d_out, int out_size, void* d_ws, size_t ws_size,
                              hipStream_t stream) {
  (void)in_sizes; (void)n_in; (void)out_size; (void)ws_size;
  hipLaunchKernelGGL(fused_decode_kernel, dim3(256), dim3(512), 0, stream, (const u16*)d_in[0], (const u16*)d_in[1], (const u16*)d_in[2],
                     (const u16*)d_in[3], (const u16*)d_in[4], (const u16*)d_in[6], (const u16*)d_in[7], (const u16*)d_in[8], (u16*)d_out,
                     (unsigned char*)d_ws);
}
```

```cpp
#define DRY_TAIL 0
#include <hip/hip_runtime.h>
#include <hip/hip_bf16.h>
#include <stdint.h>

typedef unsigned short u16;
typedef unsigned v4u __attribute__((ext_vector_type(4)));
typedef unsigned v2u __attribute__((ext_vector_type(2)));
typedef float f32x4 __attribute__((ext_vector_type(4)));
typedef float f32x2 __attribute__((ext_vector_type(2)));
typedef __bf16 bf16x2 __attribute__((ext_vector_type(2)));
typedef __amdgpu_buffer_rsrc_t rsrc_t;
typedef __attribute__((address_space(3))) volatile unsigned lds_vu32;
typedef __attribute__((address_space(3))) volatile v4u lds_vv4u;

namespace {

constexpr int DM = 512, NH = 32, SK = 8192, DH = 128, FF = 1792;
constexpr float SCALE = 0.08838834764831845f;
constexpr float LOG2E = 1.4426950408889634f;
constexpr int AUX_NT = 2;
constexpr int AUX_SC1 = 16;
constexpr unsigned TAG_STEP = 0x9E3779B9u;
constexpr unsigned SPIN_CAP = 1u << 18;
constexpr unsigned OOR = 0x7ffffff0u;

#define DIST 2
#define TAU_NS 2285
#define SA_NS 600
#define SB_NS 100
constexpr int TAU256 = TAU_NS * 256 / 40;
#define QDELAY 450
#define LAY 1
#define COMMON_ORG 1
#define YDELAY 115
#define YSLEEP 4
#define WIN_SHIFT 8
#define WIN_PHASE 64
#define WIN_START 1000
#define AUX_K 2
#define AUX_V 2
#define ABL_NOW 0

constexpr unsigned WS_EPOCH = 0;
constexpr unsigned WS_T0 = 8192;
constexpr unsigned WS_ML = 16384;
constexpr unsigned WS_P = WS_ML + 128 * 128;
constexpr unsigned WS_Y = WS_P + 4 * 60 * 8 * 1024;
constexpr unsigned WS_END = WS_Y + 4 * 64 * 64 * 64;

constexpr int L_SC = 0;
constexpr int L_XN = L_SC + 4096;
constexpr int L_QP = L_XN + 1024;
constexpr int L_OP = L_QP + 512;
constexpr int L_GU = L_OP + 512;
constexpr int L_FL = L_GU + 512;
constexpr int L_MX = L_FL + 20;
constexpr int L_TAG = L_MX + 4;
constexpr int L_END = L_TAG + 4;

__device__ __forceinline__ rsrc_t mk_rsrc(const void* p, unsigned bytes) {
  return __builtin_amdgcn_make_buffer_rsrc(const_cast<void*>(p), 0, bytes, 0x00020000);
}
template <int AUX> __device__ __forceinline__ v4u ld16(rsrc_t r, unsigned voff, unsigned soff) {
  return __builtin_amdgcn_raw_buffer_load_b128(r, voff, soff, AUX);
}
__device__ __forceinline__ float dot2(unsigned a, unsigned b, float acc) {
  return __builtin_amdgcn_fdot2_f32_bf16(__builtin_bit_cast(bf16x2, a), __builtin_bit_cast(bf16x2, b), acc, false);
}
__device__ __forceinline__ float bflo(unsigned w) { return __uint_as_float(w << 16); }
__device__ __forceinline__ float bfhi(unsigned w) { return __uint_as_float(w & 0xffff0000u); }
__device__ __forceinline__ unsigned pk_bf16(float lo, float hi) {
  bf16x2 v = {(__bf16)lo, (__bf16)hi};
  return __builtin_bit_cast(unsigned, v);
}
__device__ __forceinline__ float rbf(float x) { return (float)(__bf16)x; }
template <int CTRL> __device__ __forceinline__ float dpp(float x) {
  return __builtin_bit_cast(float, __builtin_amdgcn_mov_dpp(__builtin_bit_cast(int, x), CTRL, 0xf, 0xf, true));
}
template <int CTRL> __device__ __forceinline__ unsigned dppu(unsigned x) {
  return (unsigned)__builtin_amdgcn_mov_dpp((int)x, CTRL, 0xf, 0xf, true);
}
constexpr int XOR1 = 0xB1, XOR2 = 0x4E, XOR7 = 0x141, XOR8 = 0x128, ROR4 = 0x124, BCAST = 0x150;
__device__ __forceinline__ float swap32_add(float a, float b) {
  auto t = __builtin_amdgcn_permlane32_swap(__float_as_uint(a), __float_as_uint(b), false, false);
  return __uint_as_float(t[0]) + __uint_as_float(t[1]);
}
__device__ __forceinline__ float swap16_add(float a, float b) {
  auto t = __builtin_amdgcn_permlane16_swap(__float_as_uint(a), __float_as_uint(b), false, false);
  return __uint_as_float(t[0]) + __uint_as_float(t[1]);
}
__device__ __forceinline__ float wave_sum(float x) {
  x += dpp<XOR1>(x); x += dpp<XOR2>(x); x += dpp<XOR7>(x); x += dpp<XOR8>(x);
  x = swap16_add(x, x); x = swap32_add(x, x);
  return x;
}
__device__ __forceinline__ float wave_max(float x) {
  x = fmaxf(x, dpp<XOR1>(x)); x = fmaxf(x, dpp<XOR2>(x)); x = fmaxf(x, dpp<XOR7>(x)); x = fmaxf(x, dpp<XOR8>(x));
  auto s = __builtin_amdgcn_permlane16_swap(__float_as_uint(x), __float_as_uint(x), false, false);
  x = fmaxf(__uint_as_float(s[0]), __uint_as_float(s[1]));
  auto t = __builtin_amdgcn_permlane32_swap(__float_as_uint(x), __float_as_uint(x), false, false);
  return fmaxf(__uint_as_float(t[0]), __uint_as_float(t[1]));
}
__device__ __forceinline__ void rows_reduce8(const float (&a)[8], float& o0, float& o1) {
  const float t0 = swap32_add(a[0], a[4]), t1 = swap32_add(a[1], a[5]), t2 = swap32_add(a[2], a[6]), t3 = swap32_add(a[3], a[7]);
  o0 = swap16_add(t0, t2); o1 = swap16_add(t1, t3);
}
__device__ __forceinline__ void wg_barrier() { asm volatile("s_waitcnt lgkmcnt(0)\n\ts_barrier" ::: "memory"); }
#define SCHED_FENCE() __builtin_amdgcn_sched_barrier(0)
#define MEM_FENCE() asm volatile("" ::: "memory")
#define PIN4(a) asm volatile("" : "+v"((a)[0]), "+v"((a)[1]), "+v"((a)[2]), "+v"((a)[3]) :: "memory")
#define PIN8(a) asm volatile("" : "+v"((a)[0]), "+v"((a)[1]), "+v"((a)[2]), "+v"((a)[3]), "+v"((a)[4]), "+v"((a)[5]), "+v"((a)[6]), "+v"((a)[7]) :: "memory")
__device__ __forceinline__ unsigned perm_lo(unsigned a, unsigned b) { return __builtin_amdgcn_perm(b, a, 0x05040100u); }
__device__ __forceinline__ unsigned perm_hi(unsigned a, unsigned b) { return __builtin_amdgcn_perm(b, a, 0x07060302u); }
__device__ __forceinline__ void gemv_pair(float (&acc)[8], const v4u& ra, const v4u& rb, unsigned xp) {
#pragma unroll
  for (int k = 0; k < 4; ++k) {
    acc[2 * k] = dot2(perm_lo(ra[k], rb[k]), xp, acc[2 * k]);
    acc[2 * k + 1] = dot2(perm_hi(ra[k], rb[k]), xp, acc[2 * k + 1]);
  }
}
__device__ __forceinline__ void lds_signal(unsigned* fl, int lane) {
  asm volatile("s_waitcnt lgkmcnt(0)" ::: "memory");
  if (lane == 0) *(lds_vu32*)fl = 1u;
}
__device__ __forceinline__ void lds_wait4(unsigned* fl4) {
#pragma unroll 1
  for (unsigned spin = 0; spin < (SPIN_CAP << 4); ++spin) {
    const v4u f = *(lds_vv4u*)fl4;
    if ((f[0] & f[1] & f[2] & f[3]) != 0u) break;
  }
  MEM_FENCE();
}
__device__ __forceinline__ unsigned now32() { return (unsigned)__builtin_amdgcn_s_memrealtime(); }
template <int SLEEP> __device__ __forceinline__ void wait_until(unsigned t) {
#pragma unroll 1
  for (int sp = 0; sp < 4096; ++sp) {
    if ((int)(now32() - t) >= 0) break;
    __builtin_amdgcn_s_sleep(SLEEP);
  }
  MEM_FENCE();
  SCHED_FENCE();
}
__device__ __forceinline__ void proj_part(const v4u* W, const v2u (&xp)[4], float* dst) {
  unsigned wp[16];
#pragma unroll
  for (int k = 0; k < 4; ++k) {
    wp[2 * k] = perm_lo(W[0][k], W[1][k]); wp[2 * k + 1] = perm_hi(W[0][k], W[1][k]);
    wp[8 + 2 * k] = perm_lo(W[2][k], W[3][k]); wp[8 + 2 * k + 1] = perm_hi(W[2][k], W[3][k]);
  }
  float t[8];
#pragma unroll
  for (int k = 0; k < 8; ++k) {
    const float a0 = dot2(wp[8 + k], xp[0][1], dot2(wp[k], xp[0][0], 0.f)), a1 = dot2(wp[8 + k], xp[1][1], dot2(wp[k], xp[1][0], 0.f));
    const float a2 = dot2(wp[8 + k], xp[2][1], dot2(wp[k], xp[2][0], 0.f)), a3 = dot2(wp[8 + k], xp[3][1], dot2(wp[k], xp[3][0], 0.f));
    t[k] = swap16_add(swap32_add(a0, a2), swap32_add(a1, a3));
  }
  *reinterpret_cast<f32x4*>(dst) = f32x4{t[0], t[1], t[2], t[3]};
  *reinterpret_cast<f32x4*>(dst + 4) = f32x4{t[4], t[5], t[6], t[7]};
}

template <bool VPASS = false>
__device__ __forceinline__ void ld_quarter(v4u* R, rsrc_t rk, rsrc_t rv, unsigned voff, int n, int rot, unsigned tb) {
  if (TAU256 > 0) {
    const unsigned due = tb + (unsigned)(SA_NS / 10) + (unsigned)((n * TAU256) >> 8) + (unsigned)((n < 12 ? n : 12) * (SB_NS / 10));
#pragma unroll 1
    for (int sp = 0; sp < 512; ++sp) {
      if ((int)(now32() - due) >= 0 || n >= 128) break;
      __builtin_amdgcn_s_sleep(1);
    }
    SCHED_FENCE();
  }
  const rsrc_t rn = n < 64 ? rk : rv;
  const unsigned nr = (unsigned)(n + rot) & 63u;
  const unsigned off = n < 128 ? (LAY == 1 ? nr << 14 : ((nr >> 1) << 15) + ((nr & 1u) << 12)) : (1u << 20);
  if (AUX_K == AUX_V) {
#pragma unroll
    for (int i = 0; i < 4; ++i) R[i] = ld16<AUX_K>(rn, voff + i * 1024, off);
  } else if (VPASS) {
#pragma unroll
    for (int i = 0; i < 4; ++i) R[i] = ld16<AUX_V>(rn, voff + i * 1024, off);
  } else {
#pragma unroll
    for (int i = 0; i < 4; ++i) R[i] = ld16<AUX_K>(rn, voff + i * 1024, off);
  }
  SCHED_FENCE();
}
#define WAIT_Q() asm volatile("s_waitcnt vmcnt(4)" ::: "memory")
#define WAIT_PRO() asm volatile("s_waitcnt vmcnt(4)" ::: "memory")

template <bool REFILL, int SKIP = 0>
__device__ __forceinline__ float k_group(v4u* R, const v4u& q, int c, rsrc_t rk, rsrc_t rv, unsigned voff, int n0, int rot, unsigned tb) {
  float x[16];
#pragma unroll
  for (int k = 0; k < 4; ++k) {
    if (REFILL && k >= SKIP) {
      WAIT_Q();
      ld_quarter(R + 4 * ((k + DIST) & 3), rk, rv, voff, n0 + k + DIST, rot, tb);
    }
#pragma unroll
    for (int i = 4 * k; i < 4 * k + 4; ++i) {
      float d = dot2(R[i][0], q[0], 0.f); d = dot2(R[i][1], q[1], d); d = dot2(R[i][2], q[2], d); x[i] = dot2(R[i][3], q[3], d);
    }
    PIN4(x + 4 * k);
    SCHED_FENCE();
  }
#define TR_STEP(HALF, CTRL, BIT)                                                                 \
  _Pragma("unroll") for (int hh_ = 0; hh_ < (HALF); ++hh_) {                                     \
    const float lo_ = x[hh_], hi_ = x[hh_ + (HALF)];                                             \
    const float t1_ = lo_ + dpp<CTRL>(lo_), t2_ = hi_ + dpp<CTRL>(hi_);                          \
    x[hh_] = (c & (BIT)) ? t2_ : t1_;                                                            \
  }
  TR_STEP(8, XOR8, 8) TR_STEP(4, XOR7, 4) TR_STEP(2, XOR2, 2) TR_STEP(1, XOR1, 1)
#undef TR_STEP
  return rbf(rbf(x[0]) * SCALE);
}

template <int I> __device__ __forceinline__ void pv_one(float (&acc)[8], const v4u& V, float p) {
  const float pb = dpp<BCAST + I>(p);
#pragma unroll
  for (int k = 0; k < 4; ++k) {
    acc[2 * k] = fmaf(pb, bflo(V[k]), acc[2 * k]);
    acc[2 * k + 1] = fmaf(pb, bfhi(V[k]), acc[2 * k + 1]);
  }
}

#define STAMP(i) do { } while (0)

__global__ void __launch_bounds__(512, 2)
fused_decode_kernel(const u16* __restrict__ x, const u16* __restrict__ kc, const u16* __restrict__ vc, const u16* __restrict__ wq,
                    const u16* __restrict__ wo, const u16* __restrict__ wg, const u16* __restrict__ wu, const u16* __restrict__ wd,
                    u16* __restrict__ out, unsigned char* ws) {
  __shared__ __attribute__((aligned(16))) unsigned lds[L_END];
  float* ldsf = reinterpret_cast<float*>(lds);
  const int tid = threadIdx.x, lane = tid & 63;
  const int wave = __builtin_amdgcn_readfirstlane(tid >> 6) ^ 4;
  const int r = lane >> 4, c = lane & 15;
  const int bid = blockIdx.x, xg = bid & 7, idx = bid >> 3;
  const int hh = idx >> 3, h = xg + 8 * hh, b = (idx >> 1) & 3, s = idx & 1;
  const int bh = b * NH + h, u = h * 2 + s;
  const rsrc_t rws = mk_rsrc(ws, WS_END);
  const unsigned tb = now32();
  STAMP(0);

  if (wave < 4) {
    const size_t kvoff = ((size_t)bh * SK + (size_t)s * (SK / 2)) * DH;
    const rsrc_t rk = mk_rsrc(kc + kvoff, 1u << 20), rv = mk_rsrc(vc + kvoff, 1u << 20);
    const unsigned voff = lane * 16 + wave * (LAY == 1 ? 4096 : 8192);
    const int rot = bh * 2 + s;
    const unsigned ph = (unsigned)((((idx + xg) & 7) * TAU256) >> 11);
    const unsigned ta = tb + ph;
    v4u P[32], R[16];
    wg_barrier();
    ld_quarter(P, rk, rv, voff, 0, rot, ta);
    ld_quarter(P + 4, rk, rv, voff, 1, rot, ta);
#pragma unroll
    for (int k = 2; k < 8; ++k) { WAIT_PRO(); ld_quarter(P + 4 * k, rk, rv, voff, k, rot, ta); }
#pragma unroll
    for (int k = 0; k < 4; ++k) { WAIT_PRO(); ld_quarter(R + 4 * k, rk, rv, voff, 8 + k, rot, ta); }
    wg_barrier();
    STAMP(1);
    v4u q;
    {
      float qs[8];
#pragma unroll
      for (int k = 0; k < 8; ++k) qs[k] = 0.f;
#pragma unroll
      for (int w = 0; w < 4; ++w) {
        const f32x4 a0 = *reinterpret_cast<const f32x4*>(ldsf + L_QP + w * 128 + 8 * c);
        const f32x4 a1 = *reinterpret_cast<const f32x4*>(ldsf + L_QP + w * 128 + 8 * c + 4);
#pragma unroll
        for (int k = 0; k < 4; ++k) { qs[k] += a0[k]; qs[4 + k] += a1[k]; }
      }
#pragma unroll
      for (int k = 0; k < 4; ++k) q[k] = pk_bf16(qs[2 * k], qs[2 * k + 1]);
    }
    const unsigned tag = lds[L_TAG];
    const unsigned tc = __builtin_amdgcn_readfirstlane(lds[L_TAG + 1]) + ph;
    float* sc = ldsf + L_SC + wave * 1024 + lane;
    float mx = k_group<true, 4 - DIST>(R, q, c, rk, rv, voff, 8, rot, tc);
    sc[2 * 64] = mx;
    {
      const float sv = k_group<false>(P, q, c, rk, rv, voff, 0, rot, tc);
      sc[0] = sv;
      mx = fmaxf(mx, sv);
    }
#pragma unroll 1
    for (int g = 3; g < 16; ++g) {
      const float sv = k_group<true>(R, q, c, rk, rv, voff, 4 * g, rot, tc);
      sc[g * 64] = sv;
      mx = fmaxf(mx, sv);
      if (g == 3) {
        const float sw = k_group<false>(P + 16, q, c, rk, rv, voff, 0, rot, tc);
        sc[64] = sw;
        mx = fmaxf(mx, sw);
      }
    }
    STAMP(2);
    mx = wave_max(mx);
    if (lane == 0) ldsf[L_MX + wave] = mx;
    lds_signal(lds + L_FL + 16 + wave, lane);
    STAMP(3);
    float acc[8];
#pragma unroll
    for (int k = 0; k < 8; ++k) acc[k] = 0.f;
#pragma unroll 1
    for (int g = 0; g < 16; ++g) {
      const float p = __builtin_amdgcn_exp2f((sc[g * 64] - mx) * LOG2E);
      if (g == 10 && lane == 0) *(lds_vu32*)(lds + L_FL + 12 + wave) = 1u;
      const int n0 = 64 + 4 * g + DIST;
#define PV_Q(k)                                                                                  \
      WAIT_Q();                                                                                  \
      ld_quarter<true>(R + 4 * (((k) + DIST) & 3), rv, rv, voff, n0 + (k), rot, tc);             \
      pv_one<4 * (k)>(acc, R[4 * (k)], p); pv_one<4 * (k) + 1>(acc, R[4 * (k) + 1], p);          \
      pv_one<4 * (k) + 2>(acc, R[4 * (k) + 2], p); pv_one<4 * (k) + 3>(acc, R[4 * (k) + 3], p);  \
      PIN8(acc);                                                                                 \
      SCHED_FENCE();
      PV_Q(0) PV_Q(1) PV_Q(2) PV_Q(3)
#undef PV_Q
    }
    {
      float o0, o1;
      rows_reduce8(acc, o0, o1);
      *reinterpret_cast<f32x2*>(ldsf + L_OP + wave * 128 + 8 * c + 2 * r) = f32x2{o0, o1};
    }
    STAMP(4);
    wg_barrier();
    STAMP(5);
    if (wave != 0) return;
    const unsigned t2 = now32();
    const rsrc_t ro = mk_rsrc(out, 4 * DM * 2);
    const unsigned ooff = (unsigned)(b * DM + 8 * u + 2 * (lane & 3)) * 2u;
    const unsigned xw = __builtin_amdgcn_raw_buffer_load_b32(mk_rsrc(x, 4 * DM * 2), ooff, 0, 0);
    if (DRY_TAIL) { const unsigned touch = __builtin_amdgcn_raw_buffer_load_b32(ro, ooff, 0, 0); asm volatile("" :: "v"(touch)); }
    const unsigned ybase = WS_Y + (unsigned)(b * 64 + u) * 4096u;
#pragma unroll 1
    for (int pass = DRY_TAIL ? 0 : 1; pass < 2; ++pass) {
      const unsigned yoff = pass ? (unsigned)lane * 16u : OOR;
      if (pass) wait_until<1>(t2 + YDELAY);
      v4u g0, g1, g2, g3;
#pragma unroll 1
      for (unsigned spin = 0;; ++spin) {
        MEM_FENCE();
        g0 = ld16<AUX_SC1>(rws, yoff, ybase);
        g1 = ld16<AUX_SC1>(rws, yoff + 1024, ybase);
        g2 = ld16<AUX_SC1>(rws, yoff + 2048, ybase);
        g3 = ld16<AUX_SC1>(rws, yoff + 3072, ybase);
        const bool ok = g0[1] == tag && g0[3] == tag && g1[1] == tag && g1[3] == tag && g2[1] == tag && g2[3] == tag && g3[1] == tag && g3[3] == tag;
        if (__all(ok) || spin > SPIN_CAP || pass == 0) break;
        __builtin_amdgcn_s_sleep(YSLEEP);
      }
      float y0 = (__uint_as_float(g0[0]) + __uint_as_float(g1[0])) + (__uint_as_float(g2[0]) + __uint_as_float(g3[0]));
      float y1 = (__uint_as_float(g0[2]) + __uint_as_float(g1[2])) + (__uint_as_float(g2[2]) + __uint_as_float(g3[2]));
      y0 += dpp<ROR4>(y0); y1 += dpp<ROR4>(y1);
      y0 += dpp<XOR8>(y0); y1 += dpp<XOR8>(y1);
      y0 = swap16_add(y0, y0); y1 = swap16_add(y1, y1);
      y0 = swap32_add(y0, y0); y1 = swap32_add(y1, y1);
      __builtin_amdgcn_raw_buffer_store_b32(pk_bf16(bflo(xw) + y0, bfhi(xw) + y1), ro, (pass && lane < 4) ? ooff : OOR, 0, 0);
      __builtin_amdgcn_raw_buffer_store_b32(tag, rws, (pass && u == 0 && b == 0 && lane == 0) ? WS_EPOCH : OOR, 0, AUX_SC1);
    }
    STAMP(6);
  } else {
    const int hw = wave - 4;
    const v4u xr = ld16<0>(mk_rsrc(x + hw * DM, DM * 2), lane * 16, 0);
    const unsigned epoch = __builtin_amdgcn_raw_buffer_load_b32(rws, 0, WS_EPOCH, AUX_SC1);
    SCHED_FENCE();
    const int cq = xg * 32 + idx, pb = cq >> 3, kq = cq & 7;
    v4u W[32];
    {
      const rsrc_t rq = mk_rsrc(wq + (size_t)(64 * kq + 16 * hw) * (NH * DH) + pb * DH, 15 * 8192 + 256);
#pragma unroll
      for (int i = 0; i < 4; ++i) W[i] = ld16<AUX_NT>(rq, r * (4 * 8192) + c * 16, i * 8192);
      const u16* wf = pb < 14 ? wg + pb * 128 : wu + (pb < 28 ? pb - 14 : 0) * 128;
      const rsrc_t rf = mk_rsrc(wf + (size_t)(64 * kq + 16 * hw) * FF, (pb < 28 && !ABL_NOW) ? 15 * FF * 2 + 256 : 0);
#pragma unroll
      for (int i = 0; i < 4; ++i) W[4 + i] = ld16<AUX_NT>(rf, r * (4 * FF * 2) + c * 16, i * (FF * 2));
    }
    SCHED_FENCE();
    if (hw == 0 && lane < 20) lds[L_FL + lane] = 0u;
    wg_barrier();
    {
      float xv[8];
#pragma unroll
      for (int k = 0; k < 4; ++k) { xv[2 * k] = bflo(xr[k]); xv[2 * k + 1] = bfhi(xr[k]); }
      const float sm = ((xv[0] + xv[1]) + (xv[2] + xv[3])) + ((xv[4] + xv[5]) + (xv[6] + xv[7]));
      const float mean = wave_sum(sm) * (1.f / DM);
      float sq = 0.f;
#pragma unroll
      for (int k = 0; k < 8; ++k) { const float d = xv[k] - mean; sq = fmaf(d, d, sq); }
      const float var = wave_sum(sq) * (1.f / DM);
      const float mb = rbf(mean);
      const float sd = rbf(sqrtf(rbf(rbf(var) + 1e-5f)));
      const float ri = 1.f / sd;
      float xn[8];
#pragma unroll
      for (int k = 0; k < 8; ++k) {
        const float d = rbf(xv[k] - mb);
        const float q0 = d * ri;
        xn[k] = fmaf(fmaf(-q0, sd, d), ri, q0);
      }
      *reinterpret_cast<v4u*>(lds + L_XN + hw * 256 + 4 * lane) =
          v4u{pk_bf16(xn[0], xn[1]), pk_bf16(xn[2], xn[3]), pk_bf16(xn[4], xn[5]), pk_bf16(xn[6], xn[7])};
    }
    const unsigned tag = epoch + TAG_STEP;
    if (hw == 0 && lane == 0) lds[L_TAG] = tag;
    if (COMMON_ORG && hw == 1 && idx == 0)
      __builtin_amdgcn_raw_buffer_store_b64(v2u{tb, tag}, rws, lane < 32 ? WS_T0 + (unsigned)lane * 128u + (unsigned)xg * 8u : OOR, 0, AUX_SC1);
    lds_signal(lds + L_FL + hw, lane);
    STAMP(1);
    lds_wait4(lds + L_FL);
    {
      v2u xp[4];
#pragma unroll
      for (int bb = 0; bb < 4; ++bb) xp[bb] = *reinterpret_cast<const v2u*>(lds + L_XN + bb * 256 + 32 * kq + 8 * hw + 2 * r);
      proj_part(W, xp, ldsf + L_SC + (hw * 4 + r) * 128 + 8 * c);
      proj_part(W + 4, xp, ldsf + L_SC + 2048 + (hw * 4 + r) * 128 + 8 * c);
      lds_signal(lds + L_FL + 4 + hw, lane);
      lds_wait4(lds + L_FL + 4);
      float q0 = 0.f, q1 = 0.f, f0 = 0.f, f1 = 0.f;
#pragma unroll
      for (int w = 0; w < 4; ++w) {
        const f32x2 tq = *reinterpret_cast<const f32x2*>(ldsf + L_SC + (w * 4 + hw) * 128 + 2 * lane);
        const f32x2 tf = *reinterpret_cast<const f32x2*>(ldsf + L_SC + 2048 + (w * 4 + hw) * 128 + 2 * lane);
        q0 += tq[0]; q1 += tq[1]; f0 += tf[0]; f1 += tf[1];
      }
      __builtin_amdgcn_raw_buffer_store_b128(v4u{__float_as_uint(q0), tag, __float_as_uint(q1), tag}, rws, lane * 16,
                                             WS_P + (unsigned)((hw * 60 + pb) * 8 + kq) * 1024u, AUX_SC1);
      __builtin_amdgcn_raw_buffer_store_b128(v4u{__float_as_uint(f0), tag, __float_as_uint(f1), tag}, rws, pb < 28 ? lane * 16 : OOR,
                                             WS_P + (unsigned)((hw * 60 + 32 + (pb < 28 ? pb : 0)) * 8 + kq) * 1024u, AUX_SC1);
    }
    {
      const unsigned qb = WS_P + (unsigned)((b * 60 + h) * 8 + 2 * hw) * 1024u;
      wait_until<4>(tb + QDELAY);
      if (hw == 1) {
        const v2u t0 = __builtin_amdgcn_raw_buffer_load_b64(rws, (COMMON_ORG && lane < 8) ? WS_T0 + (unsigned)idx * 128u + (unsigned)lane * 8u : OOR, 0, AUX_SC1);
        float d = (lane < 8 && t0[1] == tag) ? (float)(int)(tb - t0[0]) : 0.f;
        d = fmaxf(d, dpp<XOR1>(d)); d = fmaxf(d, dpp<XOR2>(d)); d = fmaxf(d, dpp<XOR7>(d));
        if (lane == 0) lds[L_TAG + 1] = tb - (unsigned)min((int)d, 400);
      }
      v4u pa, pc;
#pragma unroll 1
      for (unsigned spin = 0;; ++spin) {
        MEM_FENCE();
        pa = ld16<AUX_SC1>(rws, lane * 16, qb);
        pc = ld16<AUX_SC1>(rws, lane * 16 + 1024, qb);
        if (__all(pa[1] == tag && pa[3] == tag && pc[1] == tag && pc[3] == tag) || spin > SPIN_CAP) break;
        __builtin_amdgcn_s_sleep(8);
      }
      *reinterpret_cast<f32x2*>(ldsf + L_QP + hw * 128 + 2 * lane) =
          f32x2{__uint_as_float(pa[0]) + __uint_as_float(pc[0]), __uint_as_float(pa[2]) + __uint_as_float(pc[2])};
    }
    STAMP(2);
    wg_barrier();
    unsigned hp;
    {
      const int seg = lane / 14, piece = lane - seg * 14;
      const int col = 28 * u + 2 * piece, kk = 2 * hw + (seg & 1), mat = seg >> 1;
      const unsigned o = lane < 56 ? WS_P + (unsigned)(((b * 60 + 32 + 14 * mat + (col >> 7)) * 8 + kk) * 128 + (col & 127)) * 8u : OOR;
      v4u gu;
#pragma unroll 1
      for (unsigned spin = 0;; ++spin) {
        MEM_FENCE();
        gu = ld16<AUX_SC1>(rws, o, 0);
        if (__all(lane >= 56 || (gu[1] == tag && gu[3] == tag)) || spin > SPIN_CAP || ABL_NOW) break;
        __builtin_amdgcn_s_sleep(64);
      }
      if (lane < 56) *reinterpret_cast<v2u*>(lds + L_GU + (kk * 2 + mat) * 32 + 2 * piece) = v2u{gu[0], gu[2]};
      lds_signal(lds + L_FL + 8 + hw, lane);
      lds_wait4(lds + L_FL + 8);
      const int j = 8 * r + (c & 7);
      float gg = 0.f, uu = 0.f;
#pragma unroll
      for (int k8 = 0; k8 < 8; ++k8) { gg += ldsf[L_GU + k8 * 64 + j]; uu += ldsf[L_GU + k8 * 64 + 32 + j]; }
      gg = rbf(gg); uu = rbf(uu);
      const float sg = rbf(gg / (1.f + __expf(-gg)));
      const float hv = j < 28 ? sg * uu : 0.f;
      hp = pk_bf16(hv, dpp<XOR1>(hv));
      asm volatile("" : "+v"(hp));
    }
    const unsigned wphase = (unsigned)hh * WIN_PHASE;
    const unsigned cyc0 = ((((tb + WIN_START + wphase) >> (WIN_SHIFT + 1)) + 1u) << (WIN_SHIFT + 1)) - wphase;
    float y[8];
#pragma unroll
    for (int k = 0; k < 8; ++k) y[k] = 0.f;
    {
      const rsrc_t rd = mk_rsrc(wd + (size_t)(28 * u) * DM + 128 * hw, ABL_NOW ? 0 : 27 * 1024 + 256);
#define DSTEP(J)                                                                                 \
      {                                                                                          \
        wait_until<2>(cyc0 + ((unsigned)(J) << WIN_SHIFT));                                      \
        const v4u d0 = ld16<AUX_NT>(rd, r * (8 * 1024) + c * 16, (2 * (J)) * 1024);              \
        const v4u d1 = ld16<AUX_NT>(rd, r * (8 * 1024) + c * 16, (2 * (J) + 1) * 1024);          \
        gemv_pair(y, d0, d1, dppu<BCAST + 2 * (J)>(hp));                                         \
        PIN8(y);                                                                                 \
      }
      DSTEP(0) DSTEP(1) DSTEP(2) DSTEP(3)
#undef DSTEP
      const rsrc_t ro = mk_rsrc(wo + (size_t)(h * DH) * DM + 128 * hw, ABL_NOW ? 0 : 127 * 1024 + 256);
#pragma unroll
      for (int j = 0; j < 16; ++j) {
        wait_until<2>(cyc0 + ((unsigned)(4 + j) << WIN_SHIFT));
        const v4u w0 = ld16<AUX_NT>(ro, r * (32 * 1024) + c * 16, (2 * j) * 1024);
        const v4u w1 = ld16<AUX_NT>(ro, r * (32 * 1024) + c * 16, (2 * j + 1) * 1024);
#pragma unroll
        for (int k = 0; k < 4; ++k) { W[2 * j][k] = perm_lo(w0[k], w1[k]); W[2 * j + 1][k] = perm_hi(w0[k], w1[k]); }
        asm volatile("" : "+v"(W[2 * j]), "+v"(W[2 * j + 1]) :: "memory");
        if (j == 8) {
#pragma unroll 1
          for (unsigned spin = 0; spin < SPIN_CAP; ++spin) {
            if (*(lds_vu32*)(lds + L_FL + 16 + hw) != 0u) break;
            __builtin_amdgcn_s_sleep(16);
          }
          MEM_FENCE();
          const float mw = ldsf[L_MX + hw];
          float l = 0.f;
#pragma unroll
          for (int g = 0; g < 16; ++g) l += __builtin_amdgcn_exp2f((ldsf[L_SC + hw * 1024 + g * 64 + lane] - mw) * LOG2E);
          l = wave_sum(l);
          __builtin_amdgcn_raw_buffer_store_b128(v4u{__float_as_uint(mw), tag, __float_as_uint(l), tag}, rws,
                                                 lane == 0 ? WS_ML + (unsigned)((bh * 2 + s) * 4 + hw) * 16u : OOR, 0, AUX_SC1);
        }
      }
    }
    STAMP(3);
    float f[4];
    {
#pragma unroll 1
      for (unsigned spin = 0; spin < SPIN_CAP; ++spin) {
        if (*(lds_vu32*)(lds + L_FL + 12 + hw) != 0u) break;
        __builtin_amdgcn_s_sleep(32);
      }
      const unsigned o = lane < 8 ? WS_ML + (unsigned)(bh * 8 + lane) * 16u : OOR;
      v4u ml;
#pragma unroll 1
      for (unsigned spin = 0;; ++spin) {
        MEM_FENCE();
        ml = ld16<AUX_SC1>(rws, o, 0);
        if (__all(lane >= 8 || (ml[1] == tag && ml[3] == tag)) || spin > SPIN_CAP) break;
        __builtin_amdgcn_s_sleep(64);
      }
      const float mw = lane < 8 ? __uint_as_float(ml[0]) : -INFINITY;
      float M = fmaxf(mw, dpp<XOR1>(mw)); M = fmaxf(M, dpp<XOR2>(M)); M = fmaxf(M, dpp<XOR7>(M));
      const float wt = lane < 8 ? __builtin_amdgcn_exp2f((mw - M) * LOG2E) : 0.f;
      float L = wt * (lane < 8 ? __uint_as_float(ml[2]) : 0.f);
      L += dpp<XOR1>(L); L += dpp<XOR2>(L); L += dpp<XOR7>(L);
      const float fw = wt / L;
#pragma unroll
      for (int w = 0; w < 4; ++w) f[w] = __uint_as_float(__builtin_amdgcn_readlane(__float_as_uint(fw), 4 * s + w));
      PIN8(y);
    }
    STAMP(4);
    wg_barrier();
    STAMP(5);
    {
      float o0 = 0.f, o1 = 0.f;
#pragma unroll
      for (int w = 0; w < 4; ++w) {
        const f32x2 t = *reinterpret_cast<const f32x2*>(ldsf + L_OP + w * 128 + 32 * r + 2 * c);
        o0 = fmaf(f[w], t[0], o0); o1 = fmaf(f[w], t[1], o1);
      }
      const unsigned opk = pk_bf16(o0, o1);
#define YGEMV(J)                                                                                 \
  {                                                                                              \
    const unsigned ob = dppu<BCAST + (J)>(opk);                                                  \
    _Pragma("unroll") for (int k = 0; k < 4; ++k) {                                              \
      y[2 * k] = dot2(W[2 * (J)][k], ob, y[2 * k]);                                              \
      y[2 * k + 1] = dot2(W[2 * (J) + 1][k], ob, y[2 * k + 1]);                                  \
    }                                                                                            \
  }
      YGEMV(0) YGEMV(1) YGEMV(2) YGEMV(3) YGEMV(4) YGEMV(5) YGEMV(6) YGEMV(7)
      YGEMV(8) YGEMV(9) YGEMV(10) YGEMV(11) YGEMV(12) YGEMV(13) YGEMV(14) YGEMV(15)
#undef YGEMV
      float y0, y1;
      rows_reduce8(y, y0, y1);
      const unsigned o = WS_Y + (unsigned)(b * 64 + 16 * hw + c) * 4096u + (unsigned)u * 64u + (unsigned)r * 16u;
      __builtin_amdgcn_raw_buffer_store_b128(v4u{__float_as_uint(y0), tag, __float_as_uint(y1), tag}, rws, o, 0, AUX_SC1);
      STAMP(6);
    }
  }
}

}

extern "C" void kernel_launch(void* const* d_in, const int* in_sizes, int n_in, void* d_out, int out_size, void* d_ws, size_t ws_size,
                              hipStream_t stream) {
  (void)in_sizes; (void)n_in; (void)out_size; (void)ws_size;
  hipLaunchKernelGGL(fused_decode_kernel, dim3(256), dim3(512), 0, stream, (const u16*)d_in[0], (const u16*)d_in[1], (const u16*)d_in[2],
                     (const u16*)d_in[3], (const u16*)d_in[4], (const u16*)d_in[6], (const u16*)d_in[7], (const u16*)d_in[8], (u16*)d_out,
                     (unsigned char*)d_ws);
}
```

```cpp
#define TAU_NS 2275
#define SB_NS 50
#include <hip/hip_runtime.h>
#include <hip/hip_bf16.h>
#include <stdint.h>

typedef unsigned short u16;
typedef unsigned v4u __attribute__((ext_vector_type(4)));
typedef unsigned v2u __attribute__((ext_vector_type(2)));
typedef float f32x4 __attribute__((ext_vector_type(4)));
typedef float f32x2 __attribute__((ext_vector_type(2)));
typedef __bf16 bf16x2 __attribute__((ext_vector_type(2)));
typedef __amdgpu_buffer_rsrc_t rsrc_t;
typedef __attribute__((address_space(3))) volatile unsigned lds_vu32;
typedef __attribute__((address_space(3))) volatile v4u lds_vv4u;

namespace {

constexpr int DM = 512, NH = 32, SK = 8192, DH = 128, FF = 1792;
constexpr float SCALE = 0.08838834764831845f;
constexpr float LOG2E = 1.4426950408889634f;
constexpr int AUX_NT = 2;
constexpr int AUX_SC1 = 16;
constexpr unsigned TAG_STEP = 0x9E3779B9u;
constexpr unsigned SPIN_CAP = 1u << 18;
constexpr unsigned OOR = 0x7ffffff0u;

#define DIST 2
#define SA_NS 600
constexpr int TAU256 = TAU_NS * 256 / 40;
#define QDELAY 450
#define LAY 1
#define COMMON_ORG 1
#define YDELAY 75
#define TIER 3
#define YALIGN 1
#define YSLEEP 4
#define WIN_SHIFT 8
#define WIN_PHASE 64
#define WIN_START 1000
#define AUX_K 2
#define AUX_V 2
#define ABL_NOW 0

constexpr unsigned WS_EPOCH = 0;
constexpr unsigned WS_T0 = 8192;
constexpr unsigned WS_ML = 16384;
constexpr unsigned WS_P = WS_ML + 128 * 128;
constexpr unsigned WS_Y = WS_P + 4 * 120 * 4 * 512;
constexpr unsigned WS_END = WS_Y + 4 * 64 * 64 * 64;

constexpr int L_SC = 0;
constexpr int L_XN = L_SC + 4096;
constexpr int L_QP = L_XN + 1024;
constexpr int L_OP = L_QP + 512;
constexpr int L_GU = L_OP + 512;
constexpr int L_FL = L_GU + 256;
constexpr int L_MX = L_FL + 20;
constexpr int L_TAG = L_MX + 4;
constexpr int L_END = L_TAG + 4;

__device__ __forceinline__ rsrc_t mk_rsrc(const void* p, unsigned bytes) {
  return __builtin_amdgcn_make_buffer_rsrc(const_cast<void*>(p), 0, bytes, 0x00020000);
}
template <int AUX> __device__ __forceinline__ v4u ld16(rsrc_t r, unsigned voff, unsigned soff) {
  return __builtin_amdgcn_raw_buffer_load_b128(r, voff, soff, AUX);
}
__device__ __forceinline__ float dot2(unsigned a, unsigned b, float acc) {
  return __builtin_amdgcn_fdot2_f32_bf16(__builtin_bit_cast(bf16x2, a), __builtin_bit_cast(bf16x2, b), acc, false);
}
__device__ __forceinline__ float bflo(unsigned w) { return __uint_as_float(w << 16); }
__device__ __forceinline__ float bfhi(unsigned w) { return __uint_as_float(w & 0xffff0000u); }
__device__ __forceinline__ unsigned pk_bf16(float lo, float hi) {
  bf16x2 v = {(__bf16)lo, (__bf16)hi};
  return __builtin_bit_cast(unsigned, v);
}
__device__ __forceinline__ float rbf(float x) { return (float)(__bf16)x; }
template <int CTRL> __device__ __forceinline__ float dpp(float x) {
  return __builtin_bit_cast(float, __builtin_amdgcn_mov_dpp(__builtin_bit_cast(int, x), CTRL, 0xf, 0xf, true));
}
template <int CTRL> __device__ __forceinline__ unsigned dppu(unsigned x) {
  return (unsigned)__builtin_amdgcn_mov_dpp((int)x, CTRL, 0xf, 0xf, true);
}
constexpr int XOR1 = 0xB1, XOR2 = 0x4E, XOR7 = 0x141, XOR8 = 0x128, ROR4 = 0x124, BCAST = 0x150;
__device__ __forceinline__ float swap32_add(float a, float b) {
  auto t = __builtin_amdgcn_permlane32_swap(__float_as_uint(a), __float_as_uint(b), false, false);
  return __uint_as_float(t[0]) + __uint_as_float(t[1]);
}
__device__ __forceinline__ float swap16_add(float a, float b) {
  auto t = __builtin_amdgcn_permlane16_swap(__float_as_uint(a), __float_as_uint(b), false, false);
  return __uint_as_float(t[0]) + __uint_as_float(t[1]);
}
__device__ __forceinline__ float wave_sum(float x) {
  x += dpp<XOR1>(x); x += dpp<XOR2>(x); x += dpp<XOR7>(x); x += dpp<XOR8>(x);
  x = swap16_add(x, x); x = swap32_add(x, x);
  return x;
}
__device__ __forceinline__ float wave_max(float x) {
  x = fmaxf(x, dpp<XOR1>(x)); x = fmaxf(x, dpp<XOR2>(x)); x = fmaxf(x, dpp<XOR7>(x)); x = fmaxf(x, dpp<XOR8>(x));
  auto s = __builtin_amdgcn_permlane16_swap(__float_as_uint(x), __float_as_uint(x), false, false);
  x = fmaxf(__uint_as_float(s[0]), __uint_as_float(s[1]));
  auto t = __builtin_amdgcn_permlane32_swap(__float_as_uint(x), __float_as_uint(x), false, false);
  return fmaxf(__uint_as_float(t[0]), __uint_as_float(t[1]));
}
__device__ __forceinline__ void rows_reduce8(const float (&a)[8], float& o0, float& o1) {
  const float t0 = swap32_add(a[0], a[4]), t1 = swap32_add(a[1], a[5]), t2 = swap32_add(a[2], a[6]), t3 = swap32_add(a[3], a[7]);
  o0 = swap16_add(t0, t2); o1 = swap16_add(t1, t3);
}
__device__ __forceinline__ void wg_barrier() { asm volatile("s_waitcnt lgkmcnt(0)\n\ts_barrier" ::: "memory"); }
#define SCHED_FENCE() __builtin_amdgcn_sched_barrier(0)
#define MEM_FENCE() asm volatile("" ::: "memory")
#define PIN4(a) asm volatile("" : "+v"((a)[0]), "+v"((a)[1]), "+v"((a)[2]), "+v"((a)[3]) :: "memory")
#define PIN8(a) asm volatile("" : "+v"((a)[0]), "+v"((a)[1]), "+v"((a)[2]), "+v"((a)[3]), "+v"((a)[4]), "+v"((a)[5]), "+v"((a)[6]), "+v"((a)[7]) :: "memory")
__device__ __forceinline__ unsigned perm_lo(unsigned a, unsigned b) { return __builtin_amdgcn_perm(b, a, 0x05040100u); }
__device__ __forceinline__ unsigned perm_hi(unsigned a, unsigned b) { return __builtin_amdgcn_perm(b, a, 0x07060302u); }
__device__ __forceinline__ void gemv_pair(float (&acc)[8], const v4u& ra, const v4u& rb, unsigned xp) {
#pragma unroll
  for (int k = 0; k < 4; ++k) {
    acc[2 * k] = dot2(perm_lo(ra[k], rb[k]), xp, acc[2 * k]);
    acc[2 * k + 1] = dot2(perm_hi(ra[k], rb[k]), xp, acc[2 * k + 1]);
  }
}
__device__ __forceinline__ void lds_signal(unsigned* fl, int lane) {
  asm volatile("s_waitcnt lgkmcnt(0)" ::: "memory");
  if (lane == 0) *(lds_vu32*)fl = 1u;
}
__device__ __forceinline__ void lds_wait4(unsigned* fl4) {
#pragma unroll 1
  for (unsigned spin = 0; spin < (SPIN_CAP << 4); ++spin) {
    const v4u f = *(lds_vv4u*)fl4;
    if ((f[0] & f[1] & f[2] & f[3]) != 0u) break;
  }
  MEM_FENCE();
}
__device__ __forceinline__ unsigned now32() { return (unsigned)__builtin_amdgcn_s_memrealtime(); }
__device__ __forceinline__ void sleep_to(unsigned t, int cap) {
#pragma unroll 1
  for (int sp = 0; sp < cap; ++sp) {
    const int rem = (int)(t - now32());
    if (rem <= 0) break;
    if (rem > 140) __builtin_amdgcn_s_sleep(32);
    else if (rem > 36) __builtin_amdgcn_s_sleep(8);
    else if (rem > 12) __builtin_amdgcn_s_sleep(2);
    else __builtin_amdgcn_s_sleep(0);
  }
}
template <int SLEEP> __device__ __forceinline__ void wait_until(unsigned t) {
  sleep_to(t, 4096);
  MEM_FENCE();
  SCHED_FENCE();
}
__device__ __forceinline__ f32x4 proj_part(const v4u* W, const v2u (&xp)[4], int c) {
  unsigned wp[16];
#pragma unroll
  for (int k = 0; k < 4; ++k) {
    wp[2 * k] = perm_lo(W[0][k], W[1][k]); wp[2 * k + 1] = perm_hi(W[0][k], W[1][k]);
    wp[8 + 2 * k] = perm_lo(W[2][k], W[3][k]); wp[8 + 2 * k + 1] = perm_hi(W[2][k], W[3][k]);
  }
  float t[8];
#pragma unroll
  for (int k = 0; k < 8; ++k) {
    const float a0 = dot2(wp[8 + k], xp[0][1], dot2(wp[k], xp[0][0], 0.f)), a1 = dot2(wp[8 + k], xp[1][1], dot2(wp[k], xp[1][0], 0.f));
    const float a2 = dot2(wp[8 + k], xp[2][1], dot2(wp[k], xp[2][0], 0.f)), a3 = dot2(wp[8 + k], xp[3][1], dot2(wp[k], xp[3][0], 0.f));
    t[k] = swap16_add(swap32_add(a0, a2), swap32_add(a1, a3));
  }
  f32x4 o;
#pragma unroll
  for (int k = 0; k < 4; ++k) {
    const float t1 = t[k] + dpp<XOR8>(t[k]), t2 = t[k + 4] + dpp<XOR8>(t[k + 4]);
    o[k] = (c & 8) ? t2 : t1;
  }
  return o;
}

template <bool VPASS = false>
__device__ __forceinline__ void ld_quarter(v4u* R, rsrc_t rk, rsrc_t rv, unsigned voff, int n, int rot, unsigned tb) {
  if (TAU256 > 0) {
    const unsigned due = tb + (unsigned)(SA_NS / 10) + (unsigned)((n * TAU256) >> 8) + (unsigned)((n < 12 ? n : 12) * (SB_NS / 10));
    sleep_to(due, n < 128 ? 512 : 0);
    SCHED_FENCE();
  }
  const rsrc_t rn = n < 64 ? rk : rv;
  const unsigned nr = (unsigned)(n + rot) & 63u;
  const unsigned off = n < 128 ? (LAY == 1 ? nr << 14 : ((nr >> 1) << 15) + ((nr & 1u) << 12)) : (1u << 20);
  if (AUX_K == AUX_V) {
#pragma unroll
    for (int i = 0; i < 4; ++i) R[i] = ld16<AUX_K>(rn, voff + i * 1024, off);
  } else if (VPASS) {
#pragma unroll
    for (int i = 0; i < 4; ++i) R[i] = ld16<AUX_V>(rn, voff + i * 1024, off);
  } else {
#pragma unroll
    for (int i = 0; i < 4; ++i) R[i] = ld16<AUX_K>(rn, voff + i * 1024, off);
  }
  SCHED_FENCE();
}
#define WAIT_Q() asm volatile("s_waitcnt vmcnt(4)" ::: "memory")
#define WAIT_PRO() asm volatile("s_waitcnt vmcnt(4)" ::: "memory")

template <bool REFILL, int SKIP = 0>
__device__ __forceinline__ float k_group(v4u* R, const v4u& q, int c, rsrc_t rk, rsrc_t rv, unsigned voff, int n0, int rot, unsigned tb) {
  float x[16];
#pragma unroll
  for (int k = 0; k < 4; ++k) {
    if (REFILL && k >= SKIP) {
      WAIT_Q();
      ld_quarter(R + 4 * ((k + DIST) & 3), rk, rv, voff, n0 + k + DIST, rot, tb);
    }
#pragma unroll
    for (int i = 4 * k; i < 4 * k + 4; ++i) {
      float d = dot2(R[i][0], q[0], 0.f); d = dot2(R[i][1], q[1], d); d = dot2(R[i][2], q[2], d); x[i] = dot2(R[i][3], q[3], d);
    }
    PIN4(x + 4 * k);
    SCHED_FENCE();
  }
#define TR_STEP(HALF, CTRL, BIT)                                                                 \
  _Pragma("unroll") for (int hh_ = 0; hh_ < (HALF); ++hh_) {                                     \
    const float lo_ = x[hh_], hi_ = x[hh_ + (HALF)];                                             \
    const float t1_ = lo_ + dpp<CTRL>(lo_), t2_ = hi_ + dpp<CTRL>(hi_);                          \
    x[hh_] = (c & (BIT)) ? t2_ : t1_;                                                            \
  }
  TR_STEP(8, XOR8, 8) TR_STEP(4, XOR7, 4) TR_STEP(2, XOR2, 2) TR_STEP(1, XOR1, 1)
#undef TR_STEP
  return rbf(rbf(x[0]) * SCALE);
}

template <int I> __device__ __forceinline__ void pv_one(float (&acc)[8], const v4u& V, float p) {
  const float pb = dpp<BCAST + I>(p);
#pragma unroll
  for (int k = 0; k < 4; ++k) {
    acc[2 * k] = fmaf(pb, bflo(V[k]), acc[2 * k]);
    acc[2 * k + 1] = fmaf(pb, bfhi(V[k]), acc[2 * k + 1]);
  }
}

#define STAMP(i) do { } while (0)

__global__ void __launch_bounds__(512, 2)
fused_decode_kernel(const u16* __restrict__ x, const u16* __restrict__ kc, const u16* __restrict__ vc, const u16* __restrict__ wq,
                    const u16* __restrict__ wo, const u16* __restrict__ wg, const u16* __restrict__ wu, const u16* __restrict__ wd,
                    u16* __restrict__ out, unsigned char* ws) {
  __shared__ __attribute__((aligned(16))) unsigned lds[L_END];
  float* ldsf = reinterpret_cast<float*>(lds);
  const int tid = threadIdx.x, lane = tid & 63;
  const int wave = __builtin_amdgcn_readfirstlane(tid >> 6) ^ 4;
  const int r = lane >> 4, c = lane & 15;
  const int bid = blockIdx.x, xg = bid & 7, idx = bid >> 3;
  const int hh = idx >> 3, h = xg + 8 * hh, b = (idx >> 1) & 3, s = idx & 1;
  const int bh = b * NH + h, u = h * 2 + s;
  const rsrc_t rws = mk_rsrc(ws, WS_END);
  const unsigned tb = now32();
  STAMP(0);

  if (wave < 4) {
    const size_t kvoff = ((size_t)bh * SK + (size_t)s * (SK / 2)) * DH;
    const rsrc_t rk = mk_rsrc(kc + kvoff, 1u << 20), rv = mk_rsrc(vc + kvoff, 1u << 20);
    const unsigned voff = lane * 16 + wave * (LAY == 1 ? 4096 : 8192);
    const int rot = bh * 2 + s;
    const unsigned ph = (unsigned)((((idx + xg) & 7) * TAU256) >> 11);
    const unsigned ta = tb + ph;
    v4u P[32], R[16];
    wg_barrier();
    ld_quarter(P, rk, rv, voff, 0, rot, ta);
    ld_quarter(P + 4, rk, rv, voff, 1, rot, ta);
#pragma unroll
    for (int k = 2; k < 8; ++k) { WAIT_PRO(); ld_quarter(P + 4 * k, rk, rv, voff, k, rot, ta); }
#pragma unroll
    for (int k = 0; k < 4; ++k) { WAIT_PRO(); ld_quarter(R + 4 * k, rk, rv, voff, 8 + k, rot, ta); }
    wg_barrier();
    STAMP(1);
    v4u q;
    {
      float qs[8];
#pragma unroll
      for (int k = 0; k < 8; ++k) qs[k] = 0.f;
#pragma unroll
      for (int w = 0; w < 4; ++w) {
        const f32x4 a0 = *reinterpret_cast<const f32x4*>(ldsf + L_QP + w * 128 + 8 * c);
        const f32x4 a1 = *reinterpret_cast<const f32x4*>(ldsf + L_QP + w * 128 + 8 * c + 4);
#pragma unroll
        for (int k = 0; k < 4; ++k) { qs[k] += a0[k]; qs[4 + k] += a1[k]; }
      }
#pragma unroll
      for (int k = 0; k < 4; ++k) q[k] = pk_bf16(qs[2 * k], qs[2 * k + 1]);
    }
    const unsigned tag = lds[L_TAG];
    const unsigned tc = __builtin_amdgcn_readfirstlane(lds[L_TAG + 1]) + ph;
    float* sc = ldsf + L_SC + wave * 1024 + lane;
    float mx = k_group<true, 4 - DIST>(R, q, c, rk, rv, voff, 8, rot, tc);
    sc[2 * 64] = mx;
    {
      const float sv = k_group<false>(P, q, c, rk, rv, voff, 0, rot, tc);
      sc[0] = sv;
      mx = fmaxf(mx, sv);
    }
#pragma unroll 1
    for (int g = 3; g < 16; ++g) {
      const float sv = k_group<true>(R, q, c, rk, rv, voff, 4 * g, rot, tc);
      sc[g * 64] = sv;
      mx = fmaxf(mx, sv);
      if (g == 3) {
        const float sw = k_group<false>(P + 16, q, c, rk, rv, voff, 0, rot, tc);
        sc[64] = sw;
        mx = fmaxf(mx, sw);
      }
    }
    STAMP(2);
    mx = wave_max(mx);
    if (lane == 0) ldsf[L_MX + wave] = mx;
    lds_signal(lds + L_FL + 16 + wave, lane);
    STAMP(3);
    float acc[8];
#pragma unroll
    for (int k = 0; k < 8; ++k) acc[k] = 0.f;
#pragma unroll 1
    for (int g = 0; g < 16; ++g) {
      const float p = __builtin_amdgcn_exp2f((sc[g * 64] - mx) * LOG2E);
      if (g == 10 && lane == 0) *(lds_vu32*)(lds + L_FL + 12 + wave) = 1u;
      const int n0 = 64 + 4 * g + DIST;
#define PV_Q(k)                                                                                  \
      WAIT_Q();                                                                                  \
      ld_quarter<true>(R + 4 * (((k) + DIST) & 3), rv, rv, voff, n0 + (k), rot, tc);             \
      pv_one<4 * (k)>(acc, R[4 * (k)], p); pv_one<4 * (k) + 1>(acc, R[4 * (k) + 1], p);          \
      pv_one<4 * (k) + 2>(acc, R[4 * (k) + 2], p); pv_one<4 * (k) + 3>(acc, R[4 * (k) + 3], p);  \
      PIN8(acc);                                                                                 \
      SCHED_FENCE();
      PV_Q(0) PV_Q(1) PV_Q(2) PV_Q(3)
#undef PV_Q
    }
    {
      float o0, o1;
      rows_reduce8(acc, o0, o1);
      *reinterpret_cast<f32x2*>(ldsf + L_OP + wave * 128 + 8 * c + 2 * r) = f32x2{o0, o1};
    }
    STAMP(4);
    wg_barrier();
    STAMP(5);
    if (wave != 0) return;
    const unsigned t2 = now32();
    const unsigned ooff = (unsigned)(b * DM + 8 * u + 2 * (lane & 3)) * 2u;
    const unsigned xw = __builtin_amdgcn_raw_buffer_load_b32(mk_rsrc(x, 4 * DM * 2), ooff, 0, 0);
    const unsigned ybase = WS_Y + (unsigned)(b * 64 + u) * 4096u;
    wait_until<1>(t2 + YDELAY + (YALIGN ? (unsigned)((7 * TAU256) >> 11) - ph : 0u));
    v4u g0, g1, g2, g3;
#pragma unroll 1
    for (unsigned spin = 0;; ++spin) {
      MEM_FENCE();
      g0 = ld16<AUX_SC1>(rws, lane * 16, ybase);
      g1 = ld16<AUX_SC1>(rws, lane * 16 + 1024, ybase);
      g2 = ld16<AUX_SC1>(rws, lane * 16 + 2048, ybase);
      g3 = ld16<AUX_SC1>(rws, lane * 16 + 3072, ybase);
      const bool ok = g0[1] == tag && g0[3] == tag && g1[1] == tag && g1[3] == tag && g2[1] == tag && g2[3] == tag && g3[1] == tag && g3[3] == tag;
      if (__all(ok) || spin > SPIN_CAP) break;
      __builtin_amdgcn_s_sleep(YSLEEP);
    }
    float y0 = (__uint_as_float(g0[0]) + __uint_as_float(g1[0])) + (__uint_as_float(g2[0]) + __uint_as_float(g3[0]));
    float y1 = (__uint_as_float(g0[2]) + __uint_as_float(g1[2])) + (__uint_as_float(g2[2]) + __uint_as_float(g3[2]));
    y0 += dpp<ROR4>(y0); y1 += dpp<ROR4>(y1);
    y0 += dpp<XOR8>(y0); y1 += dpp<XOR8>(y1);
    y0 = swap16_add(y0, y0); y1 = swap16_add(y1, y1);
    y0 = swap32_add(y0, y0); y1 = swap32_add(y1, y1);
    __builtin_amdgcn_raw_buffer_store_b32(pk_bf16(bflo(xw) + y0, bfhi(xw) + y1), mk_rsrc(out, 4 * DM * 2), lane < 4 ? ooff : OOR, 0, 0);
    __builtin_amdgcn_raw_buffer_store_b32(tag, rws, (u == 0 && b == 0 && lane == 0) ? WS_EPOCH : OOR, 0, AUX_SC1);
    STAMP(6);
  } else {
    const int hw = wave - 4;
    const v4u xr = ld16<0>(mk_rsrc(x + hw * DM, DM * 2), lane * 16, 0);
    const unsigned epoch = __builtin_amdgcn_raw_buffer_load_b32(rws, 0, WS_EPOCH, AUX_SC1);
    SCHED_FENCE();
    const int cq = xg * 32 + idx, pb = cq >> 2, kq = cq & 3;
    v4u W[32];
    {
      const rsrc_t rq = mk_rsrc(wq + (size_t)(128 * kq + 32 * hw) * (NH * DH) + pb * 64, 31 * 8192 + 128);
#pragma unroll
      for (int i = 0; i < 4; ++i) W[i] = ld16<AUX_NT>(rq, (lane >> 3) * (4 * 8192) + (lane & 7) * 16, i * 8192);
      const u16* wf = pb < 28 ? wg + pb * 64 : wu + (pb < 56 ? pb - 28 : 0) * 64;
      const rsrc_t rf = mk_rsrc(wf + (size_t)(128 * kq + 32 * hw) * FF, (pb < 56 && !ABL_NOW) ? 31 * FF * 2 + 128 : 0);
#pragma unroll
      for (int i = 0; i < 4; ++i) W[4 + i] = ld16<AUX_NT>(rf, (lane >> 3) * (4 * FF * 2) + (lane & 7) * 16, i * (FF * 2));
    }
    SCHED_FENCE();
    if (hw == 0 && lane < 20) lds[L_FL + lane] = 0u;
    wg_barrier();
    {
      float xv[8];
#pragma unroll
      for (int k = 0; k < 4; ++k) { xv[2 * k] = bflo(xr[k]); xv[2 * k + 1] = bfhi(xr[k]); }
      const float sm = ((xv[0] + xv[1]) + (xv[2] + xv[3])) + ((xv[4] + xv[5]) + (xv[6] + xv[7]));
      const float mean = wave_sum(sm) * (1.f / DM);
      float sq = 0.f;
#pragma unroll
      for (int k = 0; k < 8; ++k) { const float d = xv[k] - mean; sq = fmaf(d, d, sq); }
      const float var = wave_sum(sq) * (1.f / DM);
      const float mb = rbf(mean);
      const float sd = rbf(sqrtf(rbf(rbf(var) + 1e-5f)));
      const float ri = 1.f / sd;
      float xn[8];
#pragma unroll
      for (int k = 0; k < 8; ++k) {
        const float d = rbf(xv[k] - mb);
        const float q0 = d * ri;
        xn[k] = fmaf(fmaf(-q0, sd, d), ri, q0);
      }
      *reinterpret_cast<v4u*>(lds + L_XN + hw * 256 + 4 * lane) =
          v4u{pk_bf16(xn[0], xn[1]), pk_bf16(xn[2], xn[3]), pk_bf16(xn[4], xn[5]), pk_bf16(xn[6], xn[7])};
    }
    const unsigned tag = epoch + TAG_STEP;
    if (hw == 0 && lane == 0) lds[L_TAG] = tag;
    if (COMMON_ORG && hw == 1 && idx == 0)
      __builtin_amdgcn_raw_buffer_store_b64(v2u{tb, tag}, rws, lane < 32 ? WS_T0 + (unsigned)lane * 128u + (unsigned)xg * 8u : OOR, 0, AUX_SC1);
    lds_signal(lds + L_FL + hw, lane);
    STAMP(1);
    lds_wait4(lds + L_FL);
    {
      v2u xp[4];
#pragma unroll
      for (int bb = 0; bb < 4; ++bb) xp[bb] = *reinterpret_cast<const v2u*>(lds + L_XN + bb * 256 + 64 * kq + 16 * hw + 2 * (lane >> 3));
      float* px = ldsf + L_SC + (hw * 4 + r) * 128 + 8 * (c & 7) + 4 * (c >> 3);
      *reinterpret_cast<f32x4*>(px) = proj_part(W, xp, c);
      *reinterpret_cast<f32x4*>(px + 64) = proj_part(W + 4, xp, c);
      lds_signal(lds + L_FL + 4 + hw, lane);
      lds_wait4(lds + L_FL + 4);
      float p0 = 0.f, p1 = 0.f;
#pragma unroll
      for (int w = 0; w < 4; ++w) {
        const f32x2 tt = *reinterpret_cast<const f32x2*>(ldsf + L_SC + (w * 4 + hw) * 128 + 2 * lane);
        p0 += tt[0]; p1 += tt[1];
      }
      const unsigned po = WS_P + (unsigned)(((hw * 120 + (lane < 32 ? pb : 64 + pb)) * 4 + kq) * 512 + (lane & 31) * 16);
      __builtin_amdgcn_raw_buffer_store_b128(v4u{__float_as_uint(p0), tag, __float_as_uint(p1), tag}, rws, (lane < 32 || pb < 56) ? po : OOR, 0, AUX_SC1);
    }
    {
      const unsigned qb = WS_P + (unsigned)(((b * 120 + 2 * h + (lane >> 5)) * 4 + hw) * 512 + (lane & 31) * 16);
      wait_until<4>(tb + QDELAY);
      if (hw == 1) {
        const v2u t0 = __builtin_amdgcn_raw_buffer_load_b64(rws, (COMMON_ORG && lane < 8) ? WS_T0 + (unsigned)idx * 128u + (unsigned)lane * 8u : OOR, 0, AUX_SC1);
        float d = (lane < 8 && t0[1] == tag) ? (float)(int)(tb - t0[0]) : 0.f;
        d = fmaxf(d, dpp<XOR1>(d)); d = fmaxf(d, dpp<XOR2>(d)); d = fmaxf(d, dpp<XOR7>(d));
        if (lane == 0) lds[L_TAG + 1] = tb - (unsigned)min((int)d, 400);
      }
      v4u pa;
#pragma unroll 1
      for (unsigned spin = 0;; ++spin) {
        MEM_FENCE();
        pa = ld16<AUX_SC1>(rws, qb, 0);
        if (__all(pa[1] == tag && pa[3] == tag) || spin > SPIN_CAP) break;
        __builtin_amdgcn_s_sleep(8);
      }
      *reinterpret_cast<v2u*>(lds + L_QP + hw * 128 + 2 * lane) = v2u{pa[0], pa[2]};
    }
    STAMP(2);
    wg_barrier();
    unsigned hp;
    {
      const int mat = lane >= 14, piece = lane - 14 * mat;
      const int col = 28 * u + 2 * piece;
      const unsigned o = lane < 28 ? WS_P + (unsigned)(((b * 120 + 64 + 28 * mat + (col >> 6)) * 4 + hw) * 512 + (col & 63) * 8) : OOR;
      v4u gu;
#pragma unroll 1
      for (unsigned spin = 0;; ++spin) {
        MEM_FENCE();
        gu = ld16<AUX_SC1>(rws, o, 0);
        if (__all(lane >= 28 || (gu[1] == tag && gu[3] == tag)) || spin > SPIN_CAP || ABL_NOW) break;
        __builtin_amdgcn_s_sleep(64);
      }
      if (lane < 28) *reinterpret_cast<v2u*>(lds + L_GU + (hw * 2 + mat) * 32 + 2 * piece) = v2u{gu[0], gu[2]};
      lds_signal(lds + L_FL + 8 + hw, lane);
      lds_wait4(lds + L_FL + 8);
      const int j = 8 * r + (c & 7);
      float gg = 0.f, uu = 0.f;
#pragma unroll
      for (int k4 = 0; k4 < 4; ++k4) { gg += ldsf[L_GU + k4 * 64 + j]; uu += ldsf[L_GU + k4 * 64 + 32 + j]; }
      gg = rbf(gg); uu = rbf(uu);
      const float sg = rbf(gg / (1.f + __expf(-gg)));
      const float hv = j < 28 ? sg * uu : 0.f;
      hp = pk_bf16(hv, dpp<XOR1>(hv));
      asm volatile("" : "+v"(hp));
    }
    const unsigned wphase = (unsigned)hh * WIN_PHASE;
    const unsigned cyc0 = ((((tb + WIN_START + wphase) >> (WIN_SHIFT + 1)) + 1u) << (WIN_SHIFT + 1)) - wphase;
    float y[8];
#pragma unroll
    for (int k = 0; k < 8; ++k) y[k] = 0.f;
    {
      const rsrc_t rd = mk_rsrc(wd + (size_t)(28 * u) * DM + 128 * hw, ABL_NOW ? 0 : 27 * 1024 + 256);
#define DSTEP(J)                                                                                 \
      {                                                                                          \
        wait_until<2>(cyc0 + ((unsigned)(J) << WIN_SHIFT));                                      \
        const v4u d0 = ld16<AUX_NT>(rd, r * (8 * 1024) + c * 16, (2 * (J)) * 1024);              \
        const v4u d1 = ld16<AUX_NT>(rd, r * (8 * 1024) + c * 16, (2 * (J) + 1) * 1024);          \
        gemv_pair(y, d0, d1, dppu<BCAST + 2 * (J)>(hp));                                         \
        PIN8(y);                                                                                 \
      }
      DSTEP(0) DSTEP(1) DSTEP(2) DSTEP(3)
#undef DSTEP
      const rsrc_t ro = mk_rsrc(wo + (size_t)(h * DH) * DM + 128 * hw, ABL_NOW ? 0 : 127 * 1024 + 256);
#pragma unroll
      for (int j = 0; j < 16; ++j) {
        wait_until<2>(cyc0 + ((unsigned)(4 + j) << WIN_SHIFT));
        const v4u w0 = ld16<AUX_NT>(ro, r * (32 * 1024) + c * 16, (2 * j) * 1024);
        const v4u w1 = ld16<AUX_NT>(ro, r * (32 * 1024) + c * 16, (2 * j + 1) * 1024);
#pragma unroll
        for (int k = 0; k < 4; ++k) { W[2 * j][k] = perm_lo(w0[k], w1[k]); W[2 * j + 1][k] = perm_hi(w0[k], w1[k]); }
        asm volatile("" : "+v"(W[2 * j]), "+v"(W[2 * j + 1]) :: "memory");
        if (j == 8) {
#pragma unroll 1
          for (unsigned spin = 0; spin < SPIN_CAP; ++spin) {
            if (*(lds_vu32*)(lds + L_FL + 16 + hw) != 0u) break;
            __builtin_amdgcn_s_sleep(16);
          }
          MEM_FENCE();
          const float mw = ldsf[L_MX + hw];
          float l = 0.f;
#pragma unroll
          for (int g = 0; g < 16; ++g) l += __builtin_amdgcn_exp2f((ldsf[L_SC + hw * 1024 + g * 64 + lane] - mw) * LOG2E);
          l = wave_sum(l);
          __builtin_amdgcn_raw_buffer_store_b128(v4u{__float_as_uint(mw), tag, __float_as_uint(l), tag}, rws,
                                                 lane == 0 ? WS_ML + (unsigned)((bh * 2 + s) * 4 + hw) * 16u : OOR, 0, AUX_SC1);
        }
      }
    }
    STAMP(3);
    float f[4];
    {
#pragma unroll 1
      for (unsigned spin = 0; spin < SPIN_CAP; ++spin) {
        if (*(lds_vu32*)(lds + L_FL + 12 + hw) != 0u) break;
        __builtin_amdgcn_s_sleep(32);
      }
      const unsigned o = lane < 8 ? WS_ML + (unsigned)(bh * 8 + lane) * 16u : OOR;
      v4u ml;
#pragma unroll 1
      for (unsigned spin = 0;; ++spin) {
        MEM_FENCE();
        ml = ld16<AUX_SC1>(rws, o, 0);
        if (__all(lane >= 8 || (ml[1] == tag && ml[3] == tag)) || spin > SPIN_CAP) break;
        __builtin_amdgcn_s_sleep(64);
      }
      const float mw = lane < 8 ? __uint_as_float(ml[0]) : -INFINITY;
      float M = fmaxf(mw, dpp<XOR1>(mw)); M = fmaxf(M, dpp<XOR2>(M)); M = fmaxf(M, dpp<XOR7>(M));
      const float wt = lane < 8 ? __builtin_amdgcn_exp2f((mw - M) * LOG2E) : 0.f;
      float L = wt * (lane < 8 ? __uint_as_float(ml[2]) : 0.f);
      L += dpp<XOR1>(L); L += dpp<XOR2>(L); L += dpp<XOR7>(L);
      const float fw = wt / L;
#pragma unroll
      for (int w = 0; w < 4; ++w) f[w] = __uint_as_float(__builtin_amdgcn_readlane(__float_as_uint(fw), 4 * s + w));
      PIN8(y);
    }
    STAMP(4);
    wg_barrier();
    STAMP(5);
    {
      float o0 = 0.f, o1 = 0.f;
#pragma unroll
      for (int w = 0; w < 4; ++w) {
        const f32x2 t = *reinterpret_cast<const f32x2*>(ldsf + L_OP + w * 128 + 32 * r + 2 * c);
        o0 = fmaf(f[w], t[0], o0); o1 = fmaf(f[w], t[1], o1);
      }
      const unsigned opk = pk_bf16(o0, o1);
#define YGEMV(J)                                                                                 \
  {                                                                                              \
    const unsigned ob = dppu<BCAST + (J)>(opk);                                                  \
    _Pragma("unroll") for (int k = 0; k < 4; ++k) {                                              \
      y[2 * k] = dot2(W[2 * (J)][k], ob, y[2 * k]);                                              \
      y[2 * k + 1] = dot2(W[2 * (J) + 1][k], ob, y[2 * k + 1]);                                  \
    }                                                                                            \
  }
      YGEMV(0) YGEMV(1) YGEMV(2) YGEMV(3) YGEMV(4) YGEMV(5) YGEMV(6) YGEMV(7)
      YGEMV(8) YGEMV(9) YGEMV(10) YGEMV(11) YGEMV(12) YGEMV(13) YGEMV(14) YGEMV(15)
#undef YGEMV
      float y0, y1;
      rows_reduce8(y, y0, y1);
      const unsigned o = WS_Y + (unsigned)(b * 64 + 16 * hw + c) * 4096u + (unsigned)u * 64u + (unsigned)r * 16u;
      __builtin_amdgcn_raw_buffer_store_b128(v4u{__float_as_uint(y0), tag, __float_as_uint(y1), tag}, rws, o, 0, AUX_SC1);
      STAMP(6);
    }
  }
}

}

extern "C" void kernel_launch(void* const* d_in, const int* in_sizes, int n_in, void* d_out, int out_size, void* d_ws, size_t ws_size,
                              hipStream_t stream) {
  (void)in_sizes; (void)n_in; (void)out_size; (void)ws_size;
  hipLaunchKernelGGL(fused_decode_kernel, dim3(256), dim3(512), 0, stream, (const u16*)d_in[0], (const u16*)d_in[1], (const u16*)d_in[2],
                     (const u16*)d_in[3], (const u16*)d_in[4], (const u16*)d_in[6], (const u16*)d_in[7], (const u16*)d_in[8], (u16*)d_out,
                     (unsigned char*)d_ws);
}
```

```cpp
#define TAU_NS 2275
#define SB_NS 40
#include <hip/hip_runtime.h>
#include <hip/hip_bf16.h>
#include <stdint.h>

typedef unsigned short u16;
typedef unsigned v4u __attribute__((ext_vector_type(4)));
typedef unsigned v2u __attribute__((ext_vector_type(2)));
typedef float f32x4 __attribute__((ext_vector_type(4)));
typedef float f32x2 __attribute__((ext_vector_type(2)));
typedef __bf16 bf16x2 __attribute__((ext_vector_type(2)));
typedef __amdgpu_buffer_rsrc_t rsrc_t;
typedef __attribute__((address_space(3))) volatile unsigned lds_vu32;
typedef __attribute__((address_space(3))) volatile v4u lds_vv4u;

namespace {

constexpr int DM = 512, NH = 32, SK = 8192, DH = 128, FF = 1792;
constexpr float SCALE = 0.08838834764831845f;
constexpr float LOG2E = 1.4426950408889634f;
constexpr int AUX_NT = 2;
constexpr int AUX_SC1 = 16;
constexpr unsigned TAG_STEP = 0x9E3779B9u;
constexpr unsigned SPIN_CAP = 1u << 18;
constexpr unsigned OOR = 0x7ffffff0u;

#define DIST 2
#define SA_NS 600
constexpr int TAU256 = TAU_NS * 256 / 40;
#define QDELAY 450
#define LAY 1
#define COMMON_ORG 1
#define YDELAY 75
#define TIER 3
#define ORG_MODE 1
#define YALIGN 1
#define YSLEEP 4
#define WIN_SHIFT 8
#define WIN_PHASE 64
#define WIN_START 1000
#define AUX_K 2
#define AUX_V 2
#define ABL_NOW 0

constexpr unsigned WS_EPOCH = 0;
constexpr unsigned WS_T0 = 8192;
constexpr unsigned WS_ML = 16384;
constexpr unsigned WS_P = WS_ML + 128 * 128;
constexpr unsigned WS_Y = WS_P + 4 * 120 * 4 * 512;
constexpr unsigned WS_END = WS_Y + 4 * 64 * 64 * 64;

constexpr int L_SC = 0;
constexpr int L_XN = L_SC + 4096;
constexpr int L_QP = L_XN + 1024;
constexpr int L_OP = L_QP + 512;
constexpr int L_GU = L_OP + 512;
constexpr int L_FL = L_GU + 256;
constexpr int L_MX = L_FL + 20;
constexpr int L_TAG = L_MX + 4;
constexpr int L_END = L_TAG + 4;

__device__ __forceinline__ rsrc_t mk_rsrc(const void* p, unsigned bytes) {
  return __builtin_amdgcn_make_buffer_rsrc(const_cast<void*>(p), 0, bytes, 0x00020000);
}
template <int AUX> __device__ __forceinline__ v4u ld16(rsrc_t r, unsigned voff, unsigned soff) {
  return __builtin_amdgcn_raw_buffer_load_b128(r, voff, soff, AUX);
}
__device__ __forceinline__ float dot2(unsigned a, unsigned b, float acc) {
  return __builtin_amdgcn_fdot2_f32_bf16(__builtin_bit_cast(bf16x2, a), __builtin_bit_cast(bf16x2, b), acc, false);
}
__device__ __forceinline__ float bflo(unsigned w) { return __uint_as_float(w << 16); }
__device__ __forceinline__ float bfhi(unsigned w) { return __uint_as_float(w & 0xffff0000u); }
__device__ __forceinline__ unsigned pk_bf16(float lo, float hi) {
  bf16x2 v = {(__bf16)lo, (__bf16)hi};
  return __builtin_bit_cast(unsigned, v);
}
__device__ __forceinline__ float rbf(float x) { return (float)(__bf16)x; }
template <int CTRL> __device__ __forceinline__ float dpp(float x) {
  return __builtin_bit_cast(float, __builtin_amdgcn_mov_dpp(__builtin_bit_cast(int, x), CTRL, 0xf, 0xf, true));
}
template <int CTRL> __device__ __forceinline__ unsigned dppu(unsigned x) {
  return (unsigned)__builtin_amdgcn_mov_dpp((int)x, CTRL, 0xf, 0xf, true);
}
constexpr int XOR1 = 0xB1, XOR2 = 0x4E, XOR7 = 0x141, XOR8 = 0x128, ROR4 = 0x124, BCAST = 0x150;
__device__ __forceinline__ float swap32_add(float a, float b) {
  auto t = __builtin_amdgcn_permlane32_swap(__float_as_uint(a), __float_as_uint(b), false, false);
  return __uint_as_float(t[0]) + __uint_as_float(t[1]);
}
__device__ __forceinline__ float swap16_add(float a, float b) {
  auto t = __builtin_amdgcn_permlane16_swap(__float_as_uint(a), __float_as_uint(b), false, false);
  return __uint_as_float(t[0]) + __uint_as_float(t[1]);
}
__device__ __forceinline__ float wave_sum(float x) {
  x += dpp<XOR1>(x); x += dpp<XOR2>(x); x += dpp<XOR7>(x); x += dpp<XOR8>(x);
  x = swap16_add(x, x); x = swap32_add(x, x);
  return x;
}
__device__ __forceinline__ float wave_max(float x) {
  x = fmaxf(x, dpp<XOR1>(x)); x = fmaxf(x, dpp<XOR2>(x)); x = fmaxf(x, dpp<XOR7>(x)); x = fmaxf(x, dpp<XOR8>(x));
  auto s = __builtin_amdgcn_permlane16_swap(__float_as_uint(x), __float_as_uint(x), false, false);
  x = fmaxf(__uint_as_float(s[0]), __uint_as_float(s[1]));
  auto t = __builtin_amdgcn_permlane32_swap(__float_as_uint(x), __float_as_uint(x), false, false);
  return fmaxf(__uint_as_float(t[0]), __uint_as_float(t[1]));
}
__device__ __forceinline__ void rows_reduce8(const float (&a)[8], float& o0, float& o1) {
  const float t0 = swap32_add(a[0], a[4]), t1 = swap32_add(a[1], a[5]), t2 = swap32_add(a[2], a[6]), t3 = swap32_add(a[3], a[7]);
  o0 = swap16_add(t0, t2); o1 = swap16_add(t1, t3);
}
__device__ __forceinline__ void wg_barrier() { asm volatile("s_waitcnt lgkmcnt(0)\n\ts_barrier" ::: "memory"); }
#define SCHED_FENCE() __builtin_amdgcn_sched_barrier(0)
#define MEM_FENCE() asm volatile("" ::: "memory")
#define PIN4(a) asm volatile("" : "+v"((a)[0]), "+v"((a)[1]), "+v"((a)[2]), "+v"((a)[3]) :: "memory")
#define PIN8(a) asm volatile("" : "+v"((a)[0]), "+v"((a)[1]), "+v"((a)[2]), "+v"((a)[3]), "+v"((a)[4]), "+v"((a)[5]), "+v"((a)[6]), "+v"((a)[7]) :: "memory")
__device__ __forceinline__ unsigned perm_lo(unsigned a, unsigned b) { return __builtin_amdgcn_perm(b, a, 0x05040100u); }
__device__ __forceinline__ unsigned perm_hi(unsigned a, unsigned b) { return __builtin_amdgcn_perm(b, a, 0x07060302u); }
__device__ __forceinline__ void gemv_pair(float (&acc)[8], const v4u& ra, const v4u& rb, unsigned xp) {
#pragma unroll
  for (int k = 0; k < 4; ++k) {
    acc[2 * k] = dot2(perm_lo(ra[k], rb[k]), xp, acc[2 * k]);
    acc[2 * k + 1] = dot2(perm_hi(ra[k], rb[k]), xp, acc[2 * k + 1]);
  }
}
__device__ __forceinline__ void lds_signal(unsigned* fl, int lane) {
  asm volatile("s_waitcnt lgkmcnt(0)" ::: "memory");
  if (lane == 0) *(lds_vu32*)fl = 1u;
}
__device__ __forceinline__ void lds_wait4(unsigned* fl4) {
#pragma unroll 1
  for (unsigned spin = 0; spin < (SPIN_CAP << 4); ++spin) {
    const v4u f = *(lds_vv4u*)fl4;
    if ((f[0] & f[1] & f[2] & f[3]) != 0u) break;
  }
  MEM_FENCE();
}
__device__ __forceinline__ unsigned now32() { return (unsigned)__builtin_amdgcn_s_memrealtime(); }
__device__ __forceinline__ void sleep_to(unsigned t, int cap) {
#pragma unroll 1
  for (int sp = 0; sp < cap; ++sp) {
    const int rem = (int)(t - now32());
    if (rem <= 0) break;
    if (rem > 140) __builtin_amdgcn_s_sleep(32);
    else if (rem > 36) __builtin_amdgcn_s_sleep(8);
    else if (rem > 12) __builtin_amdgcn_s_sleep(2);
    else __builtin_amdgcn_s_sleep(0);
  }
}
template <int SLEEP> __device__ __forceinline__ void wait_until(unsigned t) {
  sleep_to(t, 4096);
  MEM_FENCE();
  SCHED_FENCE();
}
__device__ __forceinline__ f32x4 proj_part(const v4u* W, const v2u (&xp)[4], int c) {
  unsigned wp[16];
#pragma unroll
  for (int k = 0; k < 4; ++k) {
    wp[2 * k] = perm_lo(W[0][k], W[1][k]); wp[2 * k + 1] = perm_hi(W[0][k], W[1][k]);
    wp[8 + 2 * k] = perm_lo(W[2][k], W[3][k]); wp[8 + 2 * k + 1] = perm_hi(W[2][k], W[3][k]);
  }
  float t[8];
#pragma unroll
  for (int k = 0; k < 8; ++k) {
    const float a0 = dot2(wp[8 + k], xp[0][1], dot2(wp[k], xp[0][0], 0.f)), a1 = dot2(wp[8 + k], xp[1][1], dot2(wp[k], xp[1][0], 0.f));
    const float a2 = dot2(wp[8 + k], xp[2][1], dot2(wp[k], xp[2][0], 0.f)), a3 = dot2(wp[8 + k], xp[3][1], dot2(wp[k], xp[3][0], 0.f));
    t[k] = swap16_add(swap32_add(a0, a2), swap32_add(a1, a3));
  }
  f32x4 o;
#pragma unroll
  for (int k = 0; k < 4; ++k) {
    const float t1 = t[k] + dpp<XOR8>(t[k]), t2 = t[k + 4] + dpp<XOR8>(t[k + 4]);
    o[k] = (c & 8) ? t2 : t1;
  }
  return o;
}

template <bool VPASS = false>
__device__ __forceinline__ void ld_quarter(v4u* R, rsrc_t rk, rsrc_t rv, unsigned voff, int n, int rot, unsigned tb) {
  if (TAU256 > 0) {
    const unsigned due = tb + (unsigned)(SA_NS / 10) + (unsigned)((n * TAU256) >> 8) + (unsigned)((n < 12 ? n : 12) * (SB_NS / 10));
    sleep_to(due, n < 128 ? 512 : 0);
    SCHED_FENCE();
  }
  const rsrc_t rn = n < 64 ? rk : rv;
  const unsigned nr = (unsigned)(n + rot) & 63u;
  const unsigned off = n < 128 ? (LAY == 1 ? nr << 14 : ((nr >> 1) << 15) + ((nr & 1u) << 12)) : (1u << 20);
  if (AUX_K == AUX_V) {
#pragma unroll
    for (int i = 0; i < 4; ++i) R[i] = ld16<AUX_K>(rn, voff + i * 1024, off);
  } else if (VPASS) {
#pragma unroll
    for (int i = 0; i < 4; ++i) R[i] = ld16<AUX_V>(rn, voff + i * 1024, off);
  } else {
#pragma unroll
    for (int i = 0; i < 4; ++i) R[i] = ld16<AUX_K>(rn, voff + i * 1024, off);
  }
  SCHED_FENCE();
}
#define WAIT_Q() asm volatile("s_waitcnt vmcnt(4)" ::: "memory")
#define WAIT_PRO() asm volatile("s_waitcnt vmcnt(4)" ::: "memory")

template <bool REFILL, int SKIP = 0>
__device__ __forceinline__ float k_group(v4u* R, const v4u& q, int c, rsrc_t rk, rsrc_t rv, unsigned voff, int n0, int rot, unsigned tb) {
  float x[16];
#pragma unroll
  for (int k = 0; k < 4; ++k) {
    if (REFILL && k >= SKIP) {
      WAIT_Q();
      ld_quarter(R + 4 * ((k + DIST) & 3), rk, rv, voff, n0 + k + DIST, rot, tb);
    }
#pragma unroll
    for (int i = 4 * k; i < 4 * k + 4; ++i) {
      float d = dot2(R[i][0], q[0], 0.f); d = dot2(R[i][1], q[1], d); d = dot2(R[i][2], q[2], d); x[i] = dot2(R[i][3], q[3], d);
    }
    PIN4(x + 4 * k);
    SCHED_FENCE();
  }
#define TR_STEP(HALF, CTRL, BIT)                                                                 \
  _Pragma("unroll") for (int hh_ = 0; hh_ < (HALF); ++hh_) {                                     \
    const float lo_ = x[hh_], hi_ = x[hh_ + (HALF)];                                             \
    const float t1_ = lo_ + dpp<CTRL>(lo_), t2_ = hi_ + dpp<CTRL>(hi_);                          \
    x[hh_] = (c & (BIT)) ? t2_ : t1_;                                                            \
  }
  TR_STEP(8, XOR8, 8) TR_STEP(4, XOR7, 4) TR_STEP(2, XOR2, 2) TR_STEP(1, XOR1, 1)
#undef TR_STEP
  return rbf(rbf(x[0]) * SCALE);
}

template <int I> __device__ __forceinline__ void pv_one(float (&acc)[8], const v4u& V, float p) {
  const float pb = dpp<BCAST + I>(p);
#pragma unroll
  for (int k = 0; k < 4; ++k) {
    acc[2 * k] = fmaf(pb, bflo(V[k]), acc[2 * k]);
    acc[2 * k + 1] = fmaf(pb, bfhi(V[k]), acc[2 * k + 1]);
  }
}

#define STAMP(i) do { } while (0)

__global__ void __launch_bounds__(512, 2)
fused_decode_kernel(const u16* __restrict__ x, const u16* __restrict__ kc, const u16* __restrict__ vc, const u16* __restrict__ wq,
                    const u16* __restrict__ wo, const u16* __restrict__ wg, const u16* __restrict__ wu, const u16* __restrict__ wd,
                    u16* __restrict__ out, unsigned char* ws) {
  __shared__ __attribute__((aligned(16))) unsigned lds[L_END];
  float* ldsf = reinterpret_cast<float*>(lds);
  const int tid = threadIdx.x, lane = tid & 63;
  const int wave = __builtin_amdgcn_readfirstlane(tid >> 6) ^ 4;
  const int r = lane >> 4, c = lane & 15;
  const int bid = blockIdx.x, xg = bid & 7, idx = bid >> 3;
  const int hh = idx >> 3, h = xg + 8 * hh, b = (idx >> 1) & 3, s = idx & 1;
  const int bh = b * NH + h, u = h * 2 + s;
  const rsrc_t rws = mk_rsrc(ws, WS_END);
  const unsigned tb = now32();
  STAMP(0);

  if (wave < 4) {
    const size_t kvoff = ((size_t)bh * SK + (size_t)s * (SK / 2)) * DH;
    const rsrc_t rk = mk_rsrc(kc + kvoff, 1u << 20), rv = mk_rsrc(vc + kvoff, 1u << 20);
    const unsigned voff = lane * 16 + wave * (LAY == 1 ? 4096 : 8192);
    const int rot = bh * 2 + s;
    const unsigned ph = (unsigned)((((idx + xg) & 7) * TAU256) >> 11);
    const unsigned ta = tb + ph;
    v4u P[32], R[16];
    wg_barrier();
    ld_quarter(P, rk, rv, voff, 0, rot, ta);
    ld_quarter(P + 4, rk, rv, voff, 1, rot, ta);
#pragma unroll
    for (int k = 2; k < 8; ++k) { WAIT_PRO(); ld_quarter(P + 4 * k, rk, rv, voff, k, rot, ta); }
#pragma unroll
    for (int k = 0; k < 4; ++k) { WAIT_PRO(); ld_quarter(R + 4 * k, rk, rv, voff, 8 + k, rot, ta); }
    wg_barrier();
    STAMP(1);
    v4u q;
    {
      float qs[8];
#pragma unroll
      for (int k = 0; k < 8; ++k) qs[k] = 0.f;
#pragma unroll
      for (int w = 0; w < 4; ++w) {
        const f32x4 a0 = *reinterpret_cast<const f32x4*>(ldsf + L_QP + w * 128 + 8 * c);
        const f32x4 a1 = *reinterpret_cast<const f32x4*>(ldsf + L_QP + w * 128 + 8 * c + 4);
#pragma unroll
        for (int k = 0; k < 4; ++k) { qs[k] += a0[k]; qs[4 + k] += a1[k]; }
      }
#pragma unroll
      for (int k = 0; k < 4; ++k) q[k] = pk_bf16(qs[2 * k], qs[2 * k + 1]);
    }
    const unsigned tag = lds[L_TAG];
    const unsigned tc = __builtin_amdgcn_readfirstlane(lds[L_TAG + 1]) + ph;
    float* sc = ldsf + L_SC + wave * 1024 + lane;
    float mx = k_group<true, 4 - DIST>(R, q, c, rk, rv, voff, 8, rot, tc);
    sc[2 * 64] = mx;
    {
      const float sv = k_group<false>(P, q, c, rk, rv, voff, 0, rot, tc);
      sc[0] = sv;
      mx = fmaxf(mx, sv);
    }
#pragma unroll 1
    for (int g = 3; g < 16; ++g) {
      const float sv = k_group<true>(R, q, c, rk, rv, voff, 4 * g, rot, tc);
      sc[g * 64] = sv;
      mx = fmaxf(mx, sv);
      if (g == 3) {
        const float sw = k_group<false>(P + 16, q, c, rk, rv, voff, 0, rot, tc);
        sc[64] = sw;
        mx = fmaxf(mx, sw);
      }
    }
    STAMP(2);
    mx = wave_max(mx);
    if (lane == 0) ldsf[L_MX + wave] = mx;
    lds_signal(lds + L_FL + 16 + wave, lane);
    STAMP(3);
    float acc[8];
#pragma unroll
    for (int k = 0; k < 8; ++k) acc[k] = 0.f;
#pragma unroll 1
    for (int g = 0; g < 16; ++g) {
      const float p = __builtin_amdgcn_exp2f((sc[g * 64] - mx) * LOG2E);
      if (g == 10 && lane == 0) *(lds_vu32*)(lds + L_FL + 12 + wave) = 1u;
      const int n0 = 64 + 4 * g + DIST;
#define PV_Q(k)                                                                                  \
      WAIT_Q();                                                                                  \
      ld_quarter<true>(R + 4 * (((k) + DIST) & 3), rv, rv, voff, n0 + (k), rot, tc);             \
      pv_one<4 * (k)>(acc, R[4 * (k)], p); pv_one<4 * (k) + 1>(acc, R[4 * (k) + 1], p);          \
      pv_one<4 * (k) + 2>(acc, R[4 * (k) + 2], p); pv_one<4 * (k) + 3>(acc, R[4 * (k) + 3], p);  \
      PIN8(acc);                                                                                 \
      SCHED_FENCE();
      PV_Q(0) PV_Q(1) PV_Q(2) PV_Q(3)
#undef PV_Q
    }
    {
      float o0, o1;
      rows_reduce8(acc, o0, o1);
      *reinterpret_cast<f32x2*>(ldsf + L_OP + wave * 128 + 8 * c + 2 * r) = f32x2{o0, o1};
    }
    STAMP(4);
    wg_barrier();
    STAMP(5);
    if (wave != 0) return;
    const unsigned t2 = now32();
    const unsigned ooff = (unsigned)(b * DM + 8 * u + 2 * (lane & 3)) * 2u;
    const unsigned xw = __builtin_amdgcn_raw_buffer_load_b32(mk_rsrc(x, 4 * DM * 2), ooff, 0, 0);
    const unsigned ybase = WS_Y + (unsigned)(b * 64 + u) * 4096u;
    wait_until<1>(t2 + YDELAY + (YALIGN ? (unsigned)((7 * TAU256) >> 11) - ph : 0u));
    v4u g0, g1, g2, g3;
#pragma unroll 1
    for (unsigned spin = 0;; ++spin) {
      MEM_FENCE();
      g0 = ld16<AUX_SC1>(rws, lane * 16, ybase);
      g1 = ld16<AUX_SC1>(rws, lane * 16 + 1024, ybase);
      g2 = ld16<AUX_SC1>(rws, lane * 16 + 2048, ybase);
      g3 = ld16<AUX_SC1>(rws, lane * 16 + 3072, ybase);
      const bool ok = g0[1] == tag && g0[3] == tag && g1[1] == tag && g1[3] == tag && g2[1] == tag && g2[3] == tag && g3[1] == tag && g3[3] == tag;
      if (__all(ok) || spin > SPIN_CAP) break;
      __builtin_amdgcn_s_sleep(YSLEEP);
    }
    float y0 = (__uint_as_float(g0[0]) + __uint_as_float(g1[0])) + (__uint_as_float(g2[0]) + __uint_as_float(g3[0]));
    float y1 = (__uint_as_float(g0[2]) + __uint_as_float(g1[2])) + (__uint_as_float(g2[2]) + __uint_as_float(g3[2]));
    y0 += dpp<ROR4>(y0); y1 += dpp<ROR4>(y1);
    y0 += dpp<XOR8>(y0); y1 += dpp<XOR8>(y1);
    y0 = swap16_add(y0, y0); y1 = swap16_add(y1, y1);
    y0 = swap32_add(y0, y0); y1 = swap32_add(y1, y1);
    __builtin_amdgcn_raw_buffer_store_b32(pk_bf16(bflo(xw) + y0, bfhi(xw) + y1), mk_rsrc(out, 4 * DM * 2), lane < 4 ? ooff : OOR, 0, 0);
    __builtin_amdgcn_raw_buffer_store_b32(tag, rws, (u == 0 && b == 0 && lane == 0) ? WS_EPOCH : OOR, 0, AUX_SC1);
    STAMP(6);
  } else {
    const int hw = wave - 4;
    const v4u xr = ld16<0>(mk_rsrc(x + hw * DM, DM * 2), lane * 16, 0);
    const unsigned epoch = __builtin_amdgcn_raw_buffer_load_b32(rws, 0, WS_EPOCH, AUX_SC1);
    SCHED_FENCE();
    const int cq = xg * 32 + idx, pb = cq >> 2, kq = cq & 3;
    v4u W[32];
    {
      const rsrc_t rq = mk_rsrc(wq + (size_t)(128 * kq + 32 * hw) * (NH * DH) + pb * 64, 31 * 8192 + 128);
#pragma unroll
      for (int i = 0; i < 4; ++i) W[i] = ld16<AUX_NT>(rq, (lane >> 3) * (4 * 8192) + (lane & 7) * 16, i * 8192);
      const u16* wf = pb < 28 ? wg + pb * 64 : wu + (pb < 56 ? pb - 28 : 0) * 64;
      const rsrc_t rf = mk_rsrc(wf + (size_t)(128 * kq + 32 * hw) * FF, (pb < 56 && !ABL_NOW) ? 31 * FF * 2 + 128 : 0);
#pragma unroll
      for (int i = 0; i < 4; ++i) W[4 + i] = ld16<AUX_NT>(rf, (lane >> 3) * (4 * FF * 2) + (lane & 7) * 16, i * (FF * 2));
    }
    SCHED_FENCE();
    if (hw == 0 && lane < 20) lds[L_FL + lane] = 0u;
    wg_barrier();
    {
      float xv[8];
#pragma unroll
      for (int k = 0; k < 4; ++k) { xv[2 * k] = bflo(xr[k]); xv[2 * k + 1] = bfhi(xr[k]); }
      const float sm = ((xv[0] + xv[1]) + (xv[2] + xv[3])) + ((xv[4] + xv[5]) + (xv[6] + xv[7]));
      const float mean = wave_sum(sm) * (1.f / DM);
      float sq = 0.f;
#pragma unroll
      for (int k = 0; k < 8; ++k) { const float d = xv[k] - mean; sq = fmaf(d, d, sq); }
      const float var = wave_sum(sq) * (1.f / DM);
      const float mb = rbf(mean);
      const float sd = rbf(sqrtf(rbf(rbf(var) + 1e-5f)));
      const float ri = 1.f / sd;
      float xn[8];
#pragma unroll
      for (int k = 0; k < 8; ++k) {
        const float d = rbf(xv[k] - mb);
        const float q0 = d * ri;
        xn[k] = fmaf(fmaf(-q0, sd, d), ri, q0);
      }
      *reinterpret_cast<v4u*>(lds + L_XN + hw * 256 + 4 * lane) =
          v4u{pk_bf16(xn[0], xn[1]), pk_bf16(xn[2], xn[3]), pk_bf16(xn[4], xn[5]), pk_bf16(xn[6], xn[7])};
    }
    const unsigned tag = epoch + TAG_STEP;
    if (hw == 0 && lane == 0) lds[L_TAG] = tag;
    if (COMMON_ORG && hw == 1 && idx == 0)
      __builtin_amdgcn_raw_buffer_store_b64(v2u{tb, tag}, rws, lane < 32 ? WS_T0 + (unsigned)lane * 128u + (unsigned)xg * 8u : OOR, 0, AUX_SC1);
    lds_signal(lds + L_FL + hw, lane);
    STAMP(1);
    lds_wait4(lds + L_FL);
    {
      v2u xp[4];
#pragma unroll
      for (int bb = 0; bb < 4; ++bb) xp[bb] = *reinterpret_cast<const v2u*>(lds + L_XN + bb * 256 + 64 * kq + 16 * hw + 2 * (lane >> 3));
      float* px = ldsf + L_SC + (hw * 4 + r) * 128 + 8 * (c & 7) + 4 * (c >> 3);
      *reinterpret_cast<f32x4*>(px) = proj_part(W, xp, c);
      *reinterpret_cast<f32x4*>(px + 64) = proj_part(W + 4, xp, c);
      lds_signal(lds + L_FL + 4 + hw, lane);
      lds_wait4(lds + L_FL + 4);
      float p0 = 0.f, p1 = 0.f;
#pragma unroll
      for (int w = 0; w < 4; ++w) {
        const f32x2 tt = *reinterpret_cast<const f32x2*>(ldsf + L_SC + (w * 4 + hw) * 128 + 2 * lane);
        p0 += tt[0]; p1 += tt[1];
      }
      const unsigned po = WS_P + (unsigned)(((hw * 120 + (lane < 32 ? pb : 64 + pb)) * 4 + kq) * 512 + (lane & 31) * 16);
      __builtin_amdgcn_raw_buffer_store_b128(v4u{__float_as_uint(p0), tag, __float_as_uint(p1), tag}, rws, (lane < 32 || pb < 56) ? po : OOR, 0, AUX_SC1);
    }
    {
      const unsigned qb = WS_P + (unsigned)(((b * 120 + 2 * h + (lane >> 5)) * 4 + hw) * 512 + (lane & 31) * 16);
      wait_until<4>(tb + QDELAY);
      if (hw == 1) {
        const v2u t0 = __builtin_amdgcn_raw_buffer_load_b64(rws, (COMMON_ORG && lane < 8) ? WS_T0 + (unsigned)idx * 128u + (unsigned)lane * 8u : OOR, 0, AUX_SC1);
        const bool okt = lane < 8 && t0[1] == tag;
        const float dd = okt ? fminf(fmaxf((float)(int)(t0[0] - tb), -400.f), 400.f) : 0.f;
        if (ORG_MODE == 0) {
          float d = -dd;
          d = fmaxf(d, dpp<XOR1>(d)); d = fmaxf(d, dpp<XOR2>(d)); d = fmaxf(d, dpp<XOR7>(d));
          if (lane == 0) lds[L_TAG + 1] = tb - (unsigned)(int)d;
        } else {
          float sm = dd, cn = okt ? 1.f : 0.f;
          sm += dpp<XOR1>(sm); sm += dpp<XOR2>(sm); sm += dpp<XOR7>(sm);
          cn += dpp<XOR1>(cn); cn += dpp<XOR2>(cn); cn += dpp<XOR7>(cn);
          if (lane == 0) lds[L_TAG + 1] = tb + (unsigned)(int)(sm / fmaxf(cn, 1.f));
        }
      }
      v4u pa;
#pragma unroll 1
      for (unsigned spin = 0;; ++spin) {
        MEM_FENCE();
        pa = ld16<AUX_SC1>(rws, qb, 0);
        if (__all(pa[1] == tag && pa[3] == tag) || spin > SPIN_CAP) break;
        __builtin_amdgcn_s_sleep(8);
      }
      *reinterpret_cast<v2u*>(lds + L_QP + hw * 128 + 2 * lane) = v2u{pa[0], pa[2]};
    }
    STAMP(2);
    wg_barrier();
    unsigned hp;
    {
      const int mat = lane >= 14, piece = lane - 14 * mat;
      const int col = 28 * u + 2 * piece;
      const unsigned o = lane < 28 ? WS_P + (unsigned)(((b * 120 + 64 + 28 * mat + (col >> 6)) * 4 + hw) * 512 + (col & 63) * 8) : OOR;
      v4u gu;
#pragma unroll 1
      for (unsigned spin = 0;; ++spin) {
        MEM_FENCE();
        gu = ld16<AUX_SC1>(rws, o, 0);
        if (__all(lane >= 28 || (gu[1] == tag && gu[3] == tag)) || spin > SPIN_CAP || ABL_NOW) break;
        __builtin_amdgcn_s_sleep(64);
      }
      if (lane < 28) *reinterpret_cast<v2u*>(lds + L_GU + (hw * 2 + mat) * 32 + 2 * piece) = v2u{gu[0], gu[2]};
      lds_signal(lds + L_FL + 8 + hw, lane);
      lds_wait4(lds + L_FL + 8);
      const int j = 8 * r + (c & 7);
      float gg = 0.f, uu = 0.f;
#pragma unroll
      for (int k4 = 0; k4 < 4; ++k4) { gg += ldsf[L_GU + k4 * 64 + j]; uu += ldsf[L_GU + k4 * 64 + 32 + j]; }
      gg = rbf(gg); uu = rbf(uu);
      const float sg = rbf(gg / (1.f + __expf(-gg)));
      const float hv = j < 28 ? sg * uu : 0.f;
      hp = pk_bf16(hv, dpp<XOR1>(hv));
      asm volatile("" : "+v"(hp));
    }
    const unsigned wphase = (unsigned)hh * WIN_PHASE;
    const unsigned cyc0 = ((((tb + WIN_START + wphase) >> (WIN_SHIFT + 1)) + 1u) << (WIN_SHIFT + 1)) - wphase;
    float y[8];
#pragma unroll
    for (int k = 0; k < 8; ++k) y[k] = 0.f;
    {
      const rsrc_t rd = mk_rsrc(wd + (size_t)(28 * u) * DM + 128 * hw, ABL_NOW ? 0 : 27 * 1024 + 256);
#define DSTEP(J)                                                                                 \
      {                                                                                          \
        wait_until<2>(cyc0 + ((unsigned)(J) << WIN_SHIFT));                                      \
        const v4u d0 = ld16<AUX_NT>(rd, r * (8 * 1024) + c * 16, (2 * (J)) * 1024);              \
        const v4u d1 = ld16<AUX_NT>(rd, r * (8 * 1024) + c * 16, (2 * (J) + 1) * 1024);          \
        gemv_pair(y, d0, d1, dppu<BCAST + 2 * (J)>(hp));                                         \
        PIN8(y);                                                                                 \
      }
      DSTEP(0) DSTEP(1) DSTEP(2) DSTEP(3)
#undef DSTEP
      const rsrc_t ro = mk_rsrc(wo + (size_t)(h * DH) * DM + 128 * hw, ABL_NOW ? 0 : 127 * 1024 + 256);
#pragma unroll
      for (int j = 0; j < 16; ++j) {
        wait_until<2>(cyc0 + ((unsigned)(4 + j) << WIN_SHIFT));
        const v4u w0 = ld16<AUX_NT>(ro, r * (32 * 1024) + c * 16, (2 * j) * 1024);
        const v4u w1 = ld16<AUX_NT>(ro, r * (32 * 1024) + c * 16, (2 * j + 1) * 1024);
#pragma unroll
        for (int k = 0; k < 4; ++k) { W[2 * j][k] = perm_lo(w0[k], w1[k]); W[2 * j + 1][k] = perm_hi(w0[k], w1[k]); }
        asm volatile("" : "+v"(W[2 * j]), "+v"(W[2 * j + 1]) :: "memory");
        if (j == 8) {
#pragma unroll 1
          for (unsigned spin = 0; spin < SPIN_CAP; ++spin) {
            if (*(lds_vu32*)(lds + L_FL + 16 + hw) != 0u) break;
            __builtin_amdgcn_s_sleep(16);
          }
          MEM_FENCE();
          const float mw = ldsf[L_MX + hw];
          float l = 0.f;
#pragma unroll
          for (int g = 0; g < 16; ++g) l += __builtin_amdgcn_exp2f((ldsf[L_SC + hw * 1024 + g * 64 + lane] - mw) * LOG2E);
          l = wave_sum(l);
          __builtin_amdgcn_raw_buffer_store_b128(v4u{__float_as_uint(mw), tag, __float_as_uint(l), tag}, rws,
                                                 lane == 0 ? WS_ML + (unsigned)((bh * 2 + s) * 4 + hw) * 16u : OOR, 0, AUX_SC1);
        }
      }
    }
    STAMP(3);
    float f[4];
    {
#pragma unroll 1
      for (unsigned spin = 0; spin < SPIN_CAP; ++spin) {
        if (*(lds_vu32*)(lds + L_FL + 12 + hw) != 0u) break;
        __builtin_amdgcn_s_sleep(32);
      }
      const unsigned o = lane < 8 ? WS_ML + (unsigned)(bh * 8 + lane) * 16u : OOR;
      v4u ml;
#pragma unroll 1
      for (unsigned spin = 0;; ++spin) {
        MEM_FENCE();
        ml = ld16<AUX_SC1>(rws, o, 0);
        if (__all(lane >= 8 || (ml[1] == tag && ml[3] == tag)) || spin > SPIN_CAP) break;
        __builtin_amdgcn_s_sleep(64);
      }
      const float mw = lane < 8 ? __uint_as_float(ml[0]) : -INFINITY;
      float M = fmaxf(mw, dpp<XOR1>(mw)); M = fmaxf(M, dpp<XOR2>(M)); M = fmaxf(M, dpp<XOR7>(M));
      const float wt = lane < 8 ? __builtin_amdgcn_exp2f((mw - M) * LOG2E) : 0.f;
      float L = wt * (lane < 8 ? __uint_as_float(ml[2]) : 0.f);
      L += dpp<XOR1>(L); L += dpp<XOR2>(L); L += dpp<XOR7>(L);
      const float fw = wt / L;
#pragma unroll
      for (int w = 0; w < 4; ++w) f[w] = __uint_as_float(__builtin_amdgcn_readlane(__float_as_uint(fw), 4 * s + w));
      PIN8(y);
    }
    STAMP(4);
    wg_barrier();
    STAMP(5);
    {
      float o0 = 0.f, o1 = 0.f;
#pragma unroll
      for (int w = 0; w < 4; ++w) {
        const f32x2 t = *reinterpret_cast<const f32x2*>(ldsf + L_OP + w * 128 + 32 * r + 2 * c);
        o0 = fmaf(f[w], t[0], o0); o1 = fmaf(f[w], t[1], o1);
      }
      const unsigned opk = pk_bf16(o0, o1);
#define YGEMV(J)                                                                                 \
  {                                                                                              \
    const unsigned ob = dppu<BCAST + (J)>(opk);                                                  \
    _Pragma("unroll") for (int k = 0; k < 4; ++k) {                                              \
      y[2 * k] = dot2(W[2 * (J)][k], ob, y[2 * k]);                                              \
      y[2 * k + 1] = dot2(W[2 * (J) + 1][k], ob, y[2 * k + 1]);                                  \
    }                                                                                            \
  }
      YGEMV(0) YGEMV(1) YGEMV(2) YGEMV(3) YGEMV(4) YGEMV(5) YGEMV(6) YGEMV(7)
      YGEMV(8) YGEMV(9) YGEMV(10) YGEMV(11) YGEMV(12) YGEMV(13) YGEMV(14) YGEMV(15)
#undef YGEMV
      float y0, y1;
      rows_reduce8(y, y0, y1);
      const unsigned o = WS_Y + (unsigned)(b * 64 + 16 * hw + c) * 4096u + (unsigned)u * 64u + (unsigned)r * 16u;
      __builtin_amdgcn_raw_buffer_store_b128(v4u{__float_as_uint(y0), tag, __float_as_uint(y1), tag}, rws, o, 0, AUX_SC1);
      STAMP(6);
    }
  }
}

}

extern "C" void kernel_launch(void* const* d_in, const int* in_sizes, int n_in, void* d_out, int out_size, void* d_ws, size_t ws_size,
                              hipStream_t stream) {
  (void)in_sizes; (void)n_in; (void)out_size; (void)ws_size;
  hipLaunchKernelGGL(fused_decode_kernel, dim3(256), dim3(512), 0, stream, (const u16*)d_in[0], (const u16*)d_in[1], (const u16*)d_in[2],
                     (const u16*)d_in[3], (const u16*)d_in[4], (const u16*)d_in[6], (const u16*)d_in[7], (const u16*)d_in[8], (u16*)d_out,
                     (unsigned char*)d_ws);
}
```

```cpp
#include <hip/hip_runtime.h>
#include <hip/hip_bf16.h>
#include <stdint.h>

typedef unsigned short u16;
typedef unsigned v4u __attribute__((ext_vector_type(4)));
typedef unsigned v2u __attribute__((ext_vector_type(2)));
typedef float f32x4 __attribute__((ext_vector_type(4)));
typedef float f32x2 __attribute__((ext_vector_type(2)));
typedef __bf16 bf16x2 __attribute__((ext_vector_type(2)));
typedef __amdgpu_buffer_rsrc_t rsrc_t;
typedef __attribute__((address_space(3))) volatile unsigned lds_vu32;
typedef __attribute__((address_space(3))) volatile v4u lds_vv4u;

namespace {

constexpr int DM = 512, NH = 32, SK = 8192, DH = 128, FF = 1792;
constexpr float SCALE = 0.08838834764831845f;
constexpr float LOG2E = 1.4426950408889634f;
constexpr int AUX_NT = 2;
constexpr int AUX_SC1 = 16;
constexpr unsigned TAG_STEP = 0x9E3779B9u;
constexpr unsigned SPIN_CAP = 1u << 18;
constexpr unsigned OOR = 0x7ffffff0u;

constexpr int DIST = 2;
constexpr int TAU256 = 2285 * 256 / 40;
constexpr int QDELAY = 450;
constexpr int YDELAY = 75;
constexpr int WIN_START = 1000, WIN_PER = 272;
__device__ __forceinline__ constexpr unsigned sched_of(int n) {
  return 60u + (unsigned)((n < 12 ? n : 12) * 4) + (unsigned)((n * TAU256 - (n > 48 ? n - 48 : 0) * 154 - (n > 88 ? n - 88 : 0) * 154) >> 8);
}

constexpr unsigned WS_EPOCH = 0;
constexpr unsigned WS_T0 = 8192;
constexpr unsigned WS_ML = 16384;
constexpr unsigned WS_P = WS_ML + 128 * 128;
constexpr unsigned WS_Y = WS_P + 4 * 120 * 4 * 512;
constexpr unsigned WS_END = WS_Y + 4 * 64 * 64 * 64;

constexpr int L_SC = 0;
constexpr int L_XN = L_SC + 4096;
constexpr int L_QP = L_XN + 1024;
constexpr int L_OP = L_QP + 512;
constexpr int L_GU = L_OP + 512;
constexpr int L_FL = L_GU + 256;
constexpr int L_MX = L_FL + 20;
constexpr int L_TAG = L_MX + 4;
constexpr int L_END = L_TAG + 4;

__device__ __forceinline__ rsrc_t mk_rsrc(const void* p, unsigned bytes) {
  return __builtin_amdgcn_make_buffer_rsrc(const_cast<void*>(p), 0, bytes, 0x00020000);
}
template <int AUX> __device__ __forceinline__ v4u ld16(rsrc_t r, unsigned voff, unsigned soff) {
  return __builtin_amdgcn_raw_buffer_load_b128(r, voff, soff, AUX);
}
__device__ __forceinline__ float dot2(unsigned a, unsigned b, float acc) {
  return __builtin_amdgcn_fdot2_f32_bf16(__builtin_bit_cast(bf16x2, a), __builtin_bit_cast(bf16x2, b), acc, false);
}
__device__ __forceinline__ float bflo(unsigned w) { return __uint_as_float(w << 16); }
__device__ __forceinline__ float bfhi(unsigned w) { return __uint_as_float(w & 0xffff0000u); }
__device__ __forceinline__ unsigned pk_bf16(float lo, float hi) {
  bf16x2 v = {(__bf16)lo, (__bf16)hi};
  return __builtin_bit_cast(unsigned, v);
}
__device__ __forceinline__ float rbf(float x) { return (float)(__bf16)x; }
template <int CTRL> __device__ __forceinline__ float dpp(float x) {
  return __builtin_bit_cast(float, __builtin_amdgcn_mov_dpp(__builtin_bit_cast(int, x), CTRL, 0xf, 0xf, true));
}
template <int CTRL> __device__ __forceinline__ unsigned dppu(unsigned x) {
  return (unsigned)__builtin_amdgcn_mov_dpp((int)x, CTRL, 0xf, 0xf, true);
}
constexpr int XOR1 = 0xB1, XOR2 = 0x4E, XOR7 = 0x141, XOR8 = 0x128, ROR4 = 0x124, BCAST = 0x150;
__device__ __forceinline__ float swap32_add(float a, float b) {
  auto t = __builtin_amdgcn_permlane32_swap(__float_as_uint(a), __float_as_uint(b), false, false);
  return __uint_as_float(t[0]) + __uint_as_float(t[1]);
}
__device__ __forceinline__ float swap16_add(float a, float b) {
  auto t = __builtin_amdgcn_permlane16_swap(__float_as_uint(a), __float_as_uint(b), false, false);
  return __uint_as_float(t[0]) + __uint_as_float(t[1]);
}
__device__ __forceinline__ float wave_sum(float x) {
  x += dpp<XOR1>(x); x += dpp<XOR2>(x); x += dpp<XOR7>(x); x += dpp<XOR8>(x);
  x = swap16_add(x, x); x = swap32_add(x, x);
  return x;
}
__device__ __forceinline__ float wave_max(float x) {
  x = fmaxf(x, dpp<XOR1>(x)); x = fmaxf(x, dpp<XOR2>(x)); x = fmaxf(x, dpp<XOR7>(x)); x = fmaxf(x, dpp<XOR8>(x));
  auto s = __builtin_amdgcn_permlane16_swap(__float_as_uint(x), __float_as_uint(x), false, false);
  x = fmaxf(__uint_as_float(s[0]), __uint_as_float(s[1]));
  auto t = __builtin_amdgcn_permlane32_swap(__float_as_uint(x), __float_as_uint(x), false, false);
  return fmaxf(__uint_as_float(t[0]), __uint_as_float(t[1]));
}
__device__ __forceinline__ void rows_reduce8(const float (&a)[8], float& o0, float& o1) {
  const float t0 = swap32_add(a[0], a[4]), t1 = swap32_add(a[1], a[5]), t2 = swap32_add(a[2], a[6]), t3 = swap32_add(a[3], a[7]);
  o0 = swap16_add(t0, t2); o1 = swap16_add(t1, t3);
}
__device__ __forceinline__ void wg_barrier() { asm volatile("s_waitcnt lgkmcnt(0)\n\ts_barrier" ::: "memory"); }
#define SCHED_FENCE() __builtin_amdgcn_sched_barrier(0)
#define MEM_FENCE() asm volatile("" ::: "memory")
#define PIN4(a) asm volatile("" : "+v"((a)[0]), "+v"((a)[1]), "+v"((a)[2]), "+v"((a)[3]) :: "memory")
#define PIN8(a) asm volatile("" : "+v"((a)[0]), "+v"((a)[1]), "+v"((a)[2]), "+v"((a)[3]), "+v"((a)[4]), "+v"((a)[5]), "+v"((a)[6]), "+v"((a)[7]) :: "memory")
__device__ __forceinline__ unsigned perm_lo(unsigned a, unsigned b) { return __builtin_amdgcn_perm(b, a, 0x05040100u); }
__device__ __forceinline__ unsigned perm_hi(unsigned a, unsigned b) { return __builtin_amdgcn_perm(b, a, 0x07060302u); }
__device__ __forceinline__ void gemv_pair(float (&acc)[8], const v4u& ra, const v4u& rb, unsigned xp) {
#pragma unroll
  for (int k = 0; k < 4; ++k) {
    acc[2 * k] = dot2(perm_lo(ra[k], rb[k]), xp, acc[2 * k]);
    acc[2 * k + 1] = dot2(perm_hi(ra[k], rb[k]), xp, acc[2 * k + 1]);
  }
}
__device__ __forceinline__ void lds_signal(unsigned* fl, int lane) {
  asm volatile("s_waitcnt lgkmcnt(0)" ::: "memory");
  if (lane == 0) *(lds_vu32*)fl = 1u;
}
__device__ __forceinline__ void lds_wait4(unsigned* fl4) {
#pragma unroll 1
  for (unsigned spin = 0; spin < (SPIN_CAP << 4); ++spin) {
    const v4u f = *(lds_vv4u*)fl4;
    if ((f[0] & f[1] & f[2] & f[3]) != 0u) break;
  }
  MEM_FENCE();
}
__device__ __forceinline__ unsigned now32() { return (unsigned)__builtin_amdgcn_s_memrealtime(); }
__device__ __forceinline__ void sleep_to(unsigned t, int cap) {
#pragma unroll 1
  for (int sp = 0; sp < cap; ++sp) {
    const int rem = (int)(t - now32());
    if (rem <= 0) break;
    if (rem > 140) __builtin_amdgcn_s_sleep(32);
    else if (rem > 36) __builtin_amdgcn_s_sleep(8);
    else if (rem > 12) __builtin_amdgcn_s_sleep(2);
    else __builtin_amdgcn_s_sleep(0);
  }
}
__device__ __forceinline__ void wait_until(unsigned t) {
  sleep_to(t, 4096);
  MEM_FENCE();
  SCHED_FENCE();
}
__device__ __forceinline__ f32x4 proj_part(const v4u* W, const v2u (&xp)[4], int c) {
  unsigned wp[16];
#pragma unroll
  for (int k = 0; k < 4; ++k) {
    wp[2 * k] = perm_lo(W[0][k], W[1][k]); wp[2 * k + 1] = perm_hi(W[0][k], W[1][k]);
    wp[8 + 2 * k] = perm_lo(W[2][k], W[3][k]); wp[8 + 2 * k + 1] = perm_hi(W[2][k], W[3][k]);
  }
  float t[8];
#pragma unroll
  for (int k = 0; k < 8; ++k) {
    const float a0 = dot2(wp[8 + k], xp[0][1], dot2(wp[k], xp[0][0], 0.f)), a1 = dot2(wp[8 + k], xp[1][1], dot2(wp[k], xp[1][0], 0.f));
    const float a2 = dot2(wp[8 + k], xp[2][1], dot2(wp[k], xp[2][0], 0.f)), a3 = dot2(wp[8 + k], xp[3][1], dot2(wp[k], xp[3][0], 0.f));
    t[k] = swap16_add(swap32_add(a0, a2), swap32_add(a1, a3));
  }
  f32x4 o;
#pragma unroll
  for (int k = 0; k < 4; ++k) {
    const float t1 = t[k] + dpp<XOR8>(t[k]), t2 = t[k + 4] + dpp<XOR8>(t[k + 4]);
    o[k] = (c & 8) ? t2 : t1;
  }
  return o;
}

__device__ __forceinline__ void ld_quarter(v4u* R, rsrc_t rk, rsrc_t rv, unsigned voff, int n, int rot, unsigned org) {
  sleep_to(org + sched_of(n), n < 128 ? 512 : 0);
  SCHED_FENCE();
  const rsrc_t rn = n < 64 ? rk : rv;
  const unsigned off = n < 128 ? ((unsigned)(n + rot) & 63u) << 14 : (1u << 20);
#pragma unroll
  for (int i = 0; i < 4; ++i) R[i] = ld16<AUX_NT>(rn, voff + i * 1024, off);
  SCHED_FENCE();
}
#define WAIT_Q() asm volatile("s_waitcnt vmcnt(4)" ::: "memory")

template <bool REFILL, int SKIP = 0>
__device__ __forceinline__ float k_group(v4u* R, const v4u& q, int c, rsrc_t rk, rsrc_t rv, unsigned voff, int n0, int rot, unsigned tb) {
  float x[16];
#pragma unroll
  for (int k = 0; k < 4; ++k) {
    if (REFILL && k >= SKIP) {
      WAIT_Q();
      ld_quarter(R + 4 * ((k + DIST) & 3), rk, rv, voff, n0 + k + DIST, rot, tb);
    }
#pragma unroll
    for (int i = 4 * k; i < 4 * k + 4; ++i) {
      float d = dot2(R[i][0], q[0], 0.f); d = dot2(R[i][1], q[1], d); d = dot2(R[i][2], q[2], d); x[i] = dot2(R[i][3], q[3], d);
    }
    PIN4(x + 4 * k);
    SCHED_FENCE();
  }
#define TR_STEP(HALF, CTRL, BIT)                                                                 \
  _Pragma("unroll") for (int hh_ = 0; hh_ < (HALF); ++hh_) {                                     \
    const float lo_ = x[hh_], hi_ = x[hh_ + (HALF)];                                             \
    const float t1_ = lo_ + dpp<CTRL>(lo_), t2_ = hi_ + dpp<CTRL>(hi_);                          \
    x[hh_] = (c & (BIT)) ? t2_ : t1_;                                                            \
  }
  TR_STEP(8, XOR8, 8) TR_STEP(4, XOR7, 4) TR_STEP(2, XOR2, 2) TR_STEP(1, XOR1, 1)
#undef TR_STEP
  return rbf(rbf(x[0]) * SCALE);
}

template <int I> __device__ __forceinline__ void pv_one(float (&acc)[8], const v4u& V, float p) {
  const float pb = dpp<BCAST + I>(p);
#pragma unroll
  for (int k = 0; k < 4; ++k) {
    acc[2 * k] = fmaf(pb, bflo(V[k]), acc[2 * k]);
    acc[2 * k + 1] = fmaf(pb, bfhi(V[k]), acc[2 * k + 1]);
  }
}

__global__ void __launch_bounds__(512, 2)
fused_decode_kernel(const u16* __restrict__ x, const u16* __restrict__ kc, const u16* __restrict__ vc, const u16* __restrict__ wq,
                    const u16* __restrict__ wo, const u16* __restrict__ wg, const u16* __restrict__ wu, const u16* __restrict__ wd,
                    u16* __restrict__ out, unsigned char* ws) {
  __shared__ __attribute__((aligned(16))) unsigned lds[L_END];
  float* ldsf = reinterpret_cast<float*>(lds);
  const int tid = threadIdx.x, lane = tid & 63;
  const int wave = __builtin_amdgcn_readfirstlane(tid >> 6) ^ 4;
  const int r = lane >> 4, c = lane & 15;
  const int bid = blockIdx.x, xg = bid & 7, idx = bid >> 3;
  const int hh = idx >> 3, h = xg + 8 * hh, b = (idx >> 1) & 3, s = idx & 1;
  const int bh = b * NH + h, u = h * 2 + s;
  const rsrc_t rws = mk_rsrc(ws, WS_END);
  const unsigned tb = now32();

  if (wave < 4) {
    const size_t kvoff = ((size_t)bh * SK + (size_t)s * (SK / 2)) * DH;
    const rsrc_t rk = mk_rsrc(kc + kvoff, 1u << 20), rv = mk_rsrc(vc + kvoff, 1u << 20);
    const unsigned voff = lane * 16 + wave * 4096;
    const int rot = bh * 2 + s;
    const unsigned ph = (unsigned)((((idx + xg) & 7) * TAU256) >> 11);
    const unsigned ta = tb + ph;
    v4u P[32], R[16];
    wg_barrier();
    ld_quarter(P, rk, rv, voff, 0, rot, ta);
    ld_quarter(P + 4, rk, rv, voff, 1, rot, ta);
#pragma unroll
    for (int k = 2; k < 8; ++k) { WAIT_Q(); ld_quarter(P + 4 * k, rk, rv, voff, k, rot, ta); }
#pragma unroll
    for (int k = 0; k < 4; ++k) { WAIT_Q(); ld_quarter(R + 4 * k, rk, rv, voff, 8 + k, rot, ta); }
    wg_barrier();
    v4u q;
    {
      float qs[8];
#pragma unroll
      for (int k = 0; k < 8; ++k) qs[k] = 0.f;
#pragma unroll
      for (int w = 0; w < 4; ++w) {
        const f32x4 a0 = *reinterpret_cast<const f32x4*>(ldsf + L_QP + w * 128 + 8 * c);
        const f32x4 a1 = *reinterpret_cast<const f32x4*>(ldsf + L_QP + w * 128 + 8 * c + 4);
#pragma unroll
        for (int k = 0; k < 4; ++k) { qs[k] += a0[k]; qs[4 + k] += a1[k]; }
      }
#pragma unroll
      for (int k = 0; k < 4; ++k) q[k] = pk_bf16(qs[2 * k], qs[2 * k + 1]);
    }
    const unsigned tag = lds[L_TAG];
    const unsigned tc = __builtin_amdgcn_readfirstlane(lds[L_TAG + 1]) + ph;
    float* sc = ldsf + L_SC + wave * 1024 + lane;
    float mx = k_group<true, 4 - DIST>(R, q, c, rk, rv, voff, 8, rot, tc);
    sc[2 * 64] = mx;
    {
      const float sv = k_group<false>(P, q, c, rk, rv, voff, 0, rot, tc);
      sc[0] = sv;
      mx = fmaxf(mx, sv);
    }
#pragma unroll 1
    for (int g = 3; g < 16; ++g) {
      const float sv = k_group<true>(R, q, c, rk, rv, voff, 4 * g, rot, tc);
      sc[g * 64] = sv;
      mx = fmaxf(mx, sv);
      if (g == 3) {
        const float sw = k_group<false>(P + 16, q, c, rk, rv, voff, 0, rot, tc);
        sc[64] = sw;
        mx = fmaxf(mx, sw);
      }
    }
    mx = wave_max(mx);
    if (lane == 0) ldsf[L_MX + wave] = mx;
    lds_signal(lds + L_FL + 16 + wave, lane);
    float acc[8];
#pragma unroll
    for (int k = 0; k < 8; ++k) acc[k] = 0.f;
#pragma unroll 1
    for (int g = 0; g < 16; ++g) {
      const float p = __builtin_amdgcn_exp2f((sc[g * 64] - mx) * LOG2E);
      if (g == 10 && lane == 0) *(lds_vu32*)(lds + L_FL + 12 + wave) = 1u;
      const int n0 = 64 + 4 * g + DIST;
#define PV_Q(k)                                                                                  \
      WAIT_Q();                                                                                  \
      ld_quarter(R + 4 * (((k) + DIST) & 3), rv, rv, voff, n0 + (k), rot, tc);                   \
      pv_one<4 * (k)>(acc, R[4 * (k)], p); pv_one<4 * (k) + 1>(acc, R[4 * (k) + 1], p);          \
      pv_one<4 * (k) + 2>(acc, R[4 * (k) + 2], p); pv_one<4 * (k) + 3>(acc, R[4 * (k) + 3], p);  \
      PIN8(acc);                                                                                 \
      SCHED_FENCE();
      PV_Q(0) PV_Q(1) PV_Q(2) PV_Q(3)
#undef PV_Q
    }
    {
      float o0, o1;
      rows_reduce8(acc, o0, o1);
      *reinterpret_cast<f32x2*>(ldsf + L_OP + wave * 128 + 8 * c + 2 * r) = f32x2{o0, o1};
    }
    wg_barrier();
    if (wave != 0) return;
    const unsigned t2 = now32();
    const unsigned ooff = (unsigned)(b * DM + 8 * u + 2 * (lane & 3)) * 2u;
    const unsigned xw = __builtin_amdgcn_raw_buffer_load_b32(mk_rsrc(x, 4 * DM * 2), ooff, 0, 0);
    const unsigned ybase = WS_Y + (unsigned)(b * 64 + u) * 4096u;
    wait_until(t2 + YDELAY + (unsigned)((7 * TAU256) >> 11) - ph);
    v4u g0, g1, g2, g3;
#pragma unroll 1
    for (unsigned spin = 0;; ++spin) {
      MEM_FENCE();
      g0 = ld16<AUX_SC1>(rws, lane * 16, ybase);
      g1 = ld16<AUX_SC1>(rws, lane * 16 + 1024, ybase);
      g2 = ld16<AUX_SC1>(rws, lane * 16 + 2048, ybase);
      g3 = ld16<AUX_SC1>(rws, lane * 16 + 3072, ybase);
      const bool ok = g0[1] == tag && g0[3] == tag && g1[1] == tag && g1[3] == tag && g2[1] == tag && g2[3] == tag && g3[1] == tag && g3[3] == tag;
      if (__all(ok) || spin > SPIN_CAP) break;
      __builtin_amdgcn_s_sleep(4);
    }
    float y0 = (__uint_as_float(g0[0]) + __uint_as_float(g1[0])) + (__uint_as_float(g2[0]) + __uint_as_float(g3[0]));
    float y1 = (__uint_as_float(g0[2]) + __uint_as_float(g1[2])) + (__uint_as_float(g2[2]) + __uint_as_float(g3[2]));
    y0 += dpp<ROR4>(y0); y1 += dpp<ROR4>(y1);
    y0 += dpp<XOR8>(y0); y1 += dpp<XOR8>(y1);
    y0 = swap16_add(y0, y0); y1 = swap16_add(y1, y1);
    y0 = swap32_add(y0, y0); y1 = swap32_add(y1, y1);
    __builtin_amdgcn_raw_buffer_store_b32(pk_bf16(bflo(xw) + y0, bfhi(xw) + y1), mk_rsrc(out, 4 * DM * 2), lane < 4 ? ooff : OOR, 0, 0);
    __builtin_amdgcn_raw_buffer_store_b32(tag, rws, (u == 0 && b == 0 && lane == 0) ? WS_EPOCH : OOR, 0, AUX_SC1);
  } else {
    const int hw = wave - 4;
    const v4u xr = ld16<0>(mk_rsrc(x + hw * DM, DM * 2), lane * 16, 0);
    const unsigned epoch = __builtin_amdgcn_raw_buffer_load_b32(rws, 0, WS_EPOCH, AUX_SC1);
    SCHED_FENCE();
    const int cq = xg * 32 + idx, pb = cq >> 2, kq = cq & 3;
    v4u W[32];
    {
      const rsrc_t rq = mk_rsrc(wq + (size_t)(128 * kq + 32 * hw) * (NH * DH) + pb * 64, 31 * 8192 + 128);
#pragma unroll
      for (int i = 0; i < 4; ++i) W[i] = ld16<AUX_NT>(rq, (lane >> 3) * (4 * 8192) + (lane & 7) * 16, i * 8192);
      const u16* wf = pb < 28 ? wg + pb * 64 : wu + (pb < 56 ? pb - 28 : 0) * 64;
      const rsrc_t rf = mk_rsrc(wf + (size_t)(128 * kq + 32 * hw) * FF, pb < 56 ? 31 * FF * 2 + 128 : 0);
#pragma unroll
      for (int i = 0; i < 4; ++i) W[4 + i] = ld16<AUX_NT>(rf, (lane >> 3) * (4 * FF * 2) + (lane & 7) * 16, i * (FF * 2));
    }
    SCHED_FENCE();
    if (hw == 0 && lane < 20) lds[L_FL + lane] = 0u;
    wg_barrier();
    {
      float xv[8];
#pragma unroll
      for (int k = 0; k < 4; ++k) { xv[2 * k] = bflo(xr[k]); xv[2 * k + 1] = bfhi(xr[k]); }
      const float sm = ((xv[0] + xv[1]) + (xv[2] + xv[3])) + ((xv[4] + xv[5]) + (xv[6] + xv[7]));
      const float mean = wave_sum(sm) * (1.f / DM);
      float sq = 0.f;
#pragma unroll
      for (int k = 0; k < 8; ++k) { const float d = xv[k] - mean; sq = fmaf(d, d, sq); }
      const float var = wave_sum(sq) * (1.f / DM);
      const float mb = rbf(mean);
      const float sd = rbf(sqrtf(rbf(rbf(var) + 1e-5f)));
      const float ri = 1.f / sd;
      float xn[8];
#pragma unroll
      for (int k = 0; k < 8; ++k) {
        const float d = rbf(xv[k] - mb);
        const float q0 = d * ri;
        xn[k] = fmaf(fmaf(-q0, sd, d), ri, q0);
      }
      *reinterpret_cast<v4u*>(lds + L_XN + hw * 256 + 4 * lane) =
          v4u{pk_bf16(xn[0], xn[1]), pk_bf16(xn[2], xn[3]), pk_bf16(xn[4], xn[5]), pk_bf16(xn[6], xn[7])};
    }
    const unsigned tag = epoch + TAG_STEP;
    if (hw == 0 && lane == 0) lds[L_TAG] = tag;
    if (hw == 1 && idx == 0)
      __builtin_amdgcn_raw_buffer_store_b64(v2u{tb, tag}, rws, lane < 32 ? WS_T0 + (unsigned)lane * 128u + (unsigned)xg * 8u : OOR, 0, AUX_SC1);
    lds_signal(lds + L_FL + hw, lane);
    lds_wait4(lds + L_FL);
    {
      v2u xp[4];
#pragma unroll
      for (int bb = 0; bb < 4; ++bb) xp[bb] = *reinterpret_cast<const v2u*>(lds + L_XN + bb * 256 + 64 * kq + 16 * hw + 2 * (lane >> 3));
      float* px = ldsf + L_SC + (hw * 4 + r) * 128 + 8 * (c & 7) + 4 * (c >> 3);
      *reinterpret_cast<f32x4*>(px) = proj_part(W, xp, c);
      *reinterpret_cast<f32x4*>(px + 64) = proj_part(W + 4, xp, c);
      lds_signal(lds + L_FL + 4 + hw, lane);
      lds_wait4(lds + L_FL + 4);
      float p0 = 0.f, p1 = 0.f;
#pragma unroll
      for (int w = 0; w < 4; ++w) {
        const f32x2 tt = *reinterpret_cast<const f32x2*>(ldsf + L_SC + (w * 4 + hw) * 128 + 2 * lane);
        p0 += tt[0]; p1 += tt[1];
      }
      const unsigned po = WS_P + (unsigned)(((hw * 120 + (lane < 32 ? pb : 64 + pb)) * 4 + kq) * 512 + (lane & 31) * 16);
      __builtin_amdgcn_raw_buffer_store_b128(v4u{__float_as_uint(p0), tag, __float_as_uint(p1), tag}, rws, (lane < 32 || pb < 56) ? po : OOR, 0, AUX_SC1);
    }
    {
      const unsigned qb = WS_P + (unsigned)(((b * 120 + 2 * h + (lane >> 5)) * 4 + hw) * 512 + (lane & 31) * 16);
      wait_until(tb + QDELAY);
      const v2u t0 = __builtin_amdgcn_raw_buffer_load_b64(rws, (hw == 1 && lane < 8) ? WS_T0 + (unsigned)idx * 128u + (unsigned)lane * 8u : OOR, 0, AUX_SC1);
      v4u pa;
#pragma unroll 1
      for (unsigned spin = 0;; ++spin) {
        MEM_FENCE();
        pa = ld16<AUX_SC1>(rws, qb, 0);
        if (__all(pa[1] == tag && pa[3] == tag) || spin > SPIN_CAP) break;
        __builtin_amdgcn_s_sleep(8);
      }
      if (hw == 1) {
        const bool okt = lane < 8 && t0[1] == tag;
        const float dd = okt ? fminf(fmaxf((float)(int)(t0[0] - tb), -400.f), 400.f) : 0.f;
        float sm = dd, cn = okt ? 1.f : 0.f;
        sm += dpp<XOR1>(sm); sm += dpp<XOR2>(sm); sm += dpp<XOR7>(sm);
        cn += dpp<XOR1>(cn); cn += dpp<XOR2>(cn); cn += dpp<XOR7>(cn);
        if (lane == 0) lds[L_TAG + 1] = tb + (unsigned)(int)(sm / fmaxf(cn, 1.f));
      }
      *reinterpret_cast<v2u*>(lds + L_QP + hw * 128 + 2 * lane) = v2u{pa[0], pa[2]};
    }
    wg_barrier();
    unsigned hp;
    {
      const int mat = lane >= 14, piece = lane - 14 * mat;
      const int col = 28 * u + 2 * piece;
      const unsigned o = lane < 28 ? WS_P + (unsigned)(((b * 120 + 64 + 28 * mat + (col >> 6)) * 4 + hw) * 512 + (col & 63) * 8) : OOR;
      v4u gu;
#pragma unroll 1
      for (unsigned spin = 0;; ++spin) {
        MEM_FENCE();
        gu = ld16<AUX_SC1>(rws, o, 0);
        if (__all(lane >= 28 || (gu[1] == tag && gu[3] == tag)) || spin > SPIN_CAP) break;
        __builtin_amdgcn_s_sleep(64);
      }
      if (lane < 28) *reinterpret_cast<v2u*>(lds + L_GU + (hw * 2 + mat) * 32 + 2 * piece) = v2u{gu[0], gu[2]};
      lds_signal(lds + L_FL + 8 + hw, lane);
      lds_wait4(lds + L_FL + 8);
      const int j = 8 * r + (c & 7);
      float gg = 0.f, uu = 0.f;
#pragma unroll
      for (int k4 = 0; k4 < 4; ++k4) { gg += ldsf[L_GU + k4 * 64 + j]; uu += ldsf[L_GU + k4 * 64 + 32 + j]; }
      gg = rbf(gg); uu = rbf(uu);
      const float sg = rbf(gg / (1.f + __expf(-gg)));
      const float hv = j < 28 ? sg * uu : 0.f;
      hp = pk_bf16(hv, dpp<XOR1>(hv));
      asm volatile("" : "+v"(hp));
    }
    const unsigned cyc0 = __builtin_amdgcn_readfirstlane(lds[L_TAG + 1]) + WIN_START + (unsigned)(hh * (WIN_PER / 4));
    float y[8];
#pragma unroll
    for (int k = 0; k < 8; ++k) y[k] = 0.f;
    {
      const rsrc_t rd = mk_rsrc(wd + (size_t)(28 * u) * DM + 128 * hw, 27 * 1024 + 256);
#define DSTEP(J)                                                                                 \
      {                                                                                          \
        wait_until(cyc0 + (unsigned)((J) * WIN_PER));                                            \
        const v4u d0 = ld16<AUX_NT>(rd, r * (8 * 1024) + c * 16, (2 * (J)) * 1024);              \
        const v4u d1 = ld16<AUX_NT>(rd, r * (8 * 1024) + c * 16, (2 * (J) + 1) * 1024);          \
        gemv_pair(y, d0, d1, dppu<BCAST + 2 * (J)>(hp));                                         \
        PIN8(y);                                                                                 \
      }
      DSTEP(0) DSTEP(1) DSTEP(2) DSTEP(3)
#undef DSTEP
      const rsrc_t ro = mk_rsrc(wo + (size_t)(h * DH) * DM + 128 * hw, 127 * 1024 + 256);
#pragma unroll
      for (int j = 0; j < 16; ++j) {
        wait_until(cyc0 + (unsigned)((4 + j) * WIN_PER));
        const v4u w0 = ld16<AUX_NT>(ro, r * (32 * 1024) + c * 16, (2 * j) * 1024);
        const v4u w1 = ld16<AUX_NT>(ro, r * (32 * 1024) + c * 16, (2 * j + 1) * 1024);
#pragma unroll
        for (int k = 0; k < 4; ++k) { W[2 * j][k] = perm_lo(w0[k], w1[k]); W[2 * j + 1][k] = perm_hi(w0[k], w1[k]); }
        asm volatile("" : "+v"(W[2 * j]), "+v"(W[2 * j + 1]) :: "memory");
        if (j == 8) {
#pragma unroll 1
          for (unsigned spin = 0; spin < SPIN_CAP; ++spin) {
            if (*(lds_vu32*)(lds + L_FL + 16 + hw) != 0u) break;
            __builtin_amdgcn_s_sleep(16);
          }
          MEM_FENCE();
          const float mw = ldsf[L_MX + hw];
          float l = 0.f;
#pragma unroll
          for (int g = 0; g < 16; ++g) l += __builtin_amdgcn_exp2f((ldsf[L_SC + hw * 1024 + g * 64 + lane] - mw) * LOG2E);
          l = wave_sum(l);
          __builtin_amdgcn_raw_buffer_store_b128(v4u{__float_as_uint(mw), tag, __float_as_uint(l), tag}, rws,
                                                 lane == 0 ? WS_ML + (unsigned)((bh * 2 + s) * 4 + hw) * 16u : OOR, 0, AUX_SC1);
        }
      }
    }
    float f[4];
    {
#pragma unroll 1
      for (unsigned spin = 0; spin < SPIN_CAP; ++spin) {
        if (*(lds_vu32*)(lds + L_FL + 12 + hw) != 0u) break;
        __builtin_amdgcn_s_sleep(32);
      }
      const unsigned o = lane < 8 ? WS_ML + (unsigned)(bh * 8 + lane) * 16u : OOR;
      v4u ml;
#pragma unroll 1
      for (unsigned spin = 0;; ++spin) {
        MEM_FENCE();
        ml = ld16<AUX_SC1>(rws, o, 0);
        if (__all(lane >= 8 || (ml[1] == tag && ml[3] == tag)) || spin > SPIN_CAP) break;
        __builtin_amdgcn_s_sleep(64);
      }
      const float mw = lane < 8 ? __uint_as_float(ml[0]) : -INFINITY;
      float M = fmaxf(mw, dpp<XOR1>(mw)); M = fmaxf(M, dpp<XOR2>(M)); M = fmaxf(M, dpp<XOR7>(M));
      const float wt = lane < 8 ? __builtin_amdgcn_exp2f((mw - M) * LOG2E) : 0.f;
      float L = wt * (lane < 8 ? __uint_as_float(ml[2]) : 0.f);
      L += dpp<XOR1>(L); L += dpp<XOR2>(L); L += dpp<XOR7>(L);
      const float fw = wt / L;
#pragma unroll
      for (int w = 0; w < 4; ++w) f[w] = __uint_as_float(__builtin_amdgcn_readlane(__float_as_uint(fw), 4 * s + w));
      PIN8(y);
    }
    wg_barrier();
    {
      float o0 = 0.f, o1 = 0.f;
#pragma unroll
      for (int w = 0; w < 4; ++w) {
        const f32x2 t = *reinterpret_cast<const f32x2*>(ldsf + L_OP + w * 128 + 32 * r + 2 * c);
        o0 = fmaf(f[w], t[0], o0); o1 = fmaf(f[w], t[1], o1);
      }
      const unsigned opk = pk_bf16(o0, o1);
#define YGEMV(J)                                                                                 \
  {                                                                                              \
    const unsigned ob = dppu<BCAST + (J)>(opk);                                                  \
    _Pragma("unroll") for (int k = 0; k < 4; ++k) {                                              \
      y[2 * k] = dot2(W[2 * (J)][k], ob, y[2 * k]);                                              \
      y[2 * k + 1] = dot2(W[2 * (J) + 1][k], ob, y[2 * k + 1]);                                  \
    }                                                                                            \
  }
      YGEMV(0) YGEMV(1) YGEMV(2) YGEMV(3) YGEMV(4) YGEMV(5) YGEMV(6) YGEMV(7)
      YGEMV(8) YGEMV(9) YGEMV(10) YGEMV(11) YGEMV(12) YGEMV(13) YGEMV(14) YGEMV(15)
#undef YGEMV
      float y0, y1;
      rows_reduce8(y, y0, y1);
      const unsigned o = WS_Y + (unsigned)(b * 64 + 16 * hw + c) * 4096u + (unsigned)u * 64u + (unsigned)r * 16u;
      __builtin_amdgcn_raw_buffer_store_b128(v4u{__float_as_uint(y0), tag, __float_as_uint(y1), tag}, rws, o, 0, AUX_SC1);
    }
  }
}

}

extern "C" void kernel_launch(void* const* d_in, const int* in_sizes, int n_in, void* d_out, int out_size, void* d_ws, size_t ws_size,
                              hipStream_t stream) {
  (void)in_sizes; (void)n_in; (void)out_size; (void)ws_size;
  hipLaunchKernelGGL(fused_decode_kernel, dim3(256), dim3(512), 0, stream, (const u16*)d_in[0], (const u16*)d_in[1], (const u16*)d_in[2],
                     (const u16*)d_in[3], (const u16*)d_in[4], (const u16*)d_in[6], (const u16*)d_in[7], (const u16*)d_in[8], (u16*)d_out,
                     (unsigned char*)d_ws);
}
```

```cpp
#include <hip/hip_runtime.h>
#include <hip/hip_bf16.h>
#include <stdint.h>

typedef unsigned short u16;
typedef unsigned v4u __attribute__((ext_vector_type(4)));
typedef unsigned v2u __attribute__((ext_vector_type(2)));
typedef float f32x4 __attribute__((ext_vector_type(4)));
typedef float f32x2 __attribute__((ext_vector_type(2)));
typedef __bf16 bf16x2 __attribute__((ext_vector_type(2)));
typedef __amdgpu_buffer_rsrc_t rsrc_t;
typedef __attribute__((address_space(3))) volatile unsigned lds_vu32;
typedef __attribute__((address_space(3))) volatile v4u lds_vv4u;

namespace {

constexpr int DM = 512, NH = 32, SK = 8192, DH = 128, FF = 1792;
constexpr float SCALE = 0.08838834764831845f;
constexpr float LOG2E = 1.4426950408889634f;
constexpr int AUX_NT = 2;
constexpr int AUX_SC1 = 16;
constexpr unsigned TAG_STEP = 0x9E3779B9u;
constexpr unsigned SPIN_CAP = 1u << 18;
constexpr unsigned OOR = 0x7ffffff0u;

constexpr int DIST = 2;
#define TAU_NS 2256
#define SC4 5
#define SB1 6
constexpr int TAU256 = TAU_NS * 256 / 40;
constexpr int QDELAY = 450;
constexpr int YDELAY = 75;
constexpr int WIN_START = 1000, WIN_PER = 272;
__device__ __forceinline__ constexpr unsigned sched_of(int n) {
  return 60u + (unsigned)((n < 12 ? n : 12) * SB1 + (n < 14 ? 0 : n < 17 ? n - 13 : 4) * SC4) + (unsigned)((n * TAU256 - (n > 112 ? n - 112 : 0) * 205) >> 8);
}

constexpr unsigned WS_EPOCH = 0;
constexpr unsigned WS_T0 = 8192;
constexpr unsigned WS_ML = 16384;
constexpr unsigned WS_P = WS_ML + 128 * 128;
constexpr unsigned WS_Y = WS_P + 4 * 120 * 4 * 512;
constexpr unsigned WS_END = WS_Y + 4 * 64 * 64 * 64;

constexpr int L_SC = 0;
constexpr int L_XN = L_SC + 4096;
constexpr int L_QP = L_XN + 1024;
constexpr int L_OP = L_QP + 512;
constexpr int L_GU = L_OP + 512;
constexpr int L_FL = L_GU + 256;
constexpr int L_MX = L_FL + 20;
constexpr int L_TAG = L_MX + 4;
constexpr int L_END = L_TAG + 4;

__device__ __forceinline__ rsrc_t mk_rsrc(const void* p, unsigned bytes) {
  return __builtin_amdgcn_make_buffer_rsrc(const_cast<void*>(p), 0, bytes, 0x00020000);
}
template <int AUX> __device__ __forceinline__ v4u ld16(rsrc_t r, unsigned voff, unsigned soff) {
  return __builtin_amdgcn_raw_buffer_load_b128(r, voff, soff, AUX);
}
__device__ __forceinline__ float dot2(unsigned a, unsigned b, float acc) {
  return __builtin_amdgcn_fdot2_f32_bf16(__builtin_bit_cast(bf16x2, a), __builtin_bit_cast(bf16x2, b), acc, false);
}
__device__ __forceinline__ float bflo(unsigned w) { return __uint_as_float(w << 16); }
__device__ __forceinline__ float bfhi(unsigned w) { return __uint_as_float(w & 0xffff0000u); }
__device__ __forceinline__ unsigned pk_bf16(float lo, float hi) {
  bf16x2 v = {(__bf16)lo, (__bf16)hi};
  return __builtin_bit_cast(unsigned, v);
}
__device__ __forceinline__ float rbf(float x) { return (float)(__bf16)x; }
template <int CTRL> __device__ __forceinline__ float dpp(float x) {
  return __builtin_bit_cast(float, __builtin_amdgcn_mov_dpp(__builtin_bit_cast(int, x), CTRL, 0xf, 0xf, true));
}
template <int CTRL> __device__ __forceinline__ unsigned dppu(unsigned x) {
  return (unsigned)__builtin_amdgcn_mov_dpp((int)x, CTRL, 0xf, 0xf, true);
}
constexpr int XOR1 = 0xB1, XOR2 = 0x4E, XOR7 = 0x141, XOR8 = 0x128, ROR4 = 0x124, BCAST = 0x150;
__device__ __forceinline__ float swap32_add(float a, float b) {
  auto t = __builtin_amdgcn_permlane32_swap(__float_as_uint(a), __float_as_uint(b), false, false);
  return __uint_as_float(t[0]) + __uint_as_float(t[1]);
}
__device__ __forceinline__ float swap16_add(float a, float b) {
  auto t = __builtin_amdgcn_permlane16_swap(__float_as_uint(a), __float_as_uint(b), false, false);
  return __uint_as_float(t[0]) + __uint_as_float(t[1]);
}
__device__ __forceinline__ float wave_sum(float x) {
  x += dpp<XOR1>(x); x += dpp<XOR2>(x); x += dpp<XOR7>(x); x += dpp<XOR8>(x);
  x = swap16_add(x, x); x = swap32_add(x, x);
  return x;
}
__device__ __forceinline__ float wave_max(float x) {
  x = fmaxf(x, dpp<XOR1>(x)); x = fmaxf(x, dpp<XOR2>(x)); x = fmaxf(x, dpp<XOR7>(x)); x = fmaxf(x, dpp<XOR8>(x));
  auto s = __builtin_amdgcn_permlane16_swap(__float_as_uint(x), __float_as_uint(x), false, false);
  x = fmaxf(__uint_as_float(s[0]), __uint_as_float(s[1]));
  auto t = __builtin_amdgcn_permlane32_swap(__float_as_uint(x), __float_as_uint(x), false, false);
  return fmaxf(__uint_as_float(t[0]), __uint_as_float(t[1]));
}
__device__ __forceinline__ void rows_reduce8(const float (&a)[8], float& o0, float& o1) {
  const float t0 = swap32_add(a[0], a[4]), t1 = swap32_add(a[1], a[5]), t2 = swap32_add(a[2], a[6]), t3 = swap32_add(a[3], a[7]);
  o0 = swap16_add(t0, t2); o1 = swap16_add(t1, t3);
}
__device__ __forceinline__ void wg_barrier() { asm volatile("s_waitcnt lgkmcnt(0)\n\ts_barrier" ::: "memory"); }
#define SCHED_FENCE() __builtin_amdgcn_sched_barrier(0)
#define MEM_FENCE() asm volatile("" ::: "memory")
#define PIN4(a) asm volatile("" : "+v"((a)[0]), "+v"((a)[1]), "+v"((a)[2]), "+v"((a)[3]) :: "memory")
#define PIN8(a) asm volatile("" : "+v"((a)[0]), "+v"((a)[1]), "+v"((a)[2]), "+v"((a)[3]), "+v"((a)[4]), "+v"((a)[5]), "+v"((a)[6]), "+v"((a)[7]) :: "memory")
__device__ __forceinline__ unsigned perm_lo(unsigned a, unsigned b) { return __builtin_amdgcn_perm(b, a, 0x05040100u); }
__device__ __forceinline__ unsigned perm_hi(unsigned a, unsigned b) { return __builtin_amdgcn_perm(b, a, 0x07060302u); }
__device__ __forceinline__ void gemv_pair(float (&acc)[8], const v4u& ra, const v4u& rb, unsigned xp) {
#pragma unroll
  for (int k = 0; k < 4; ++k) {
    acc[2 * k] = dot2(perm_lo(ra[k], rb[k]), xp, acc[2 * k]);
    acc[2 * k + 1] = dot2(perm_hi(ra[k], rb[k]), xp, acc[2 * k + 1]);
  }
}
__device__ __forceinline__ void lds_signal(unsigned* fl, int lane) {
  asm volatile("s_waitcnt lgkmcnt(0)" ::: "memory");
  if (lane == 0) *(lds_vu32*)fl = 1u;
}
__device__ __forceinline__ void lds_wait4(unsigned* fl4) {
#pragma unroll 1
  for (unsigned spin = 0; spin < (SPIN_CAP << 4); ++spin) {
    const v4u f = *(lds_vv4u*)fl4;
    if ((f[0] & f[1] & f[2] & f[3]) != 0u) break;
  }
  MEM_FENCE();
}
__device__ __forceinline__ unsigned now32() { return (unsigned)__builtin_amdgcn_s_memrealtime(); }
__device__ __forceinline__ void sleep_to(unsigned t, int cap) {
#pragma unroll 1
  for (int sp = 0; sp < cap; ++sp) {
    const int rem = (int)(t - now32());
    if (rem <= 0) break;
    if (rem > 140) __builtin_amdgcn_s_sleep(32);
    else if (rem > 36) __builtin_amdgcn_s_sleep(8);
    else if (rem > 12) __builtin_amdgcn_s_sleep(2);
    else __builtin_amdgcn_s_sleep(0);
  }
}
__device__ __forceinline__ void wait_until(unsigned t) {
  sleep_to(t, 4096);
  MEM_FENCE();
  SCHED_FENCE();
}
__device__ __forceinline__ f32x4 proj_part(const v4u* W, const v2u (&xp)[4], int c) {
  unsigned wp[16];
#pragma unroll
  for (int k = 0; k < 4; ++k) {
    wp[2 * k] = perm_lo(W[0][k], W[1][k]); wp[2 * k + 1] = perm_hi(W[0][k], W[1][k]);
    wp[8 + 2 * k] = perm_lo(W[2][k], W[3][k]); wp[8 + 2 * k + 1] = perm_hi(W[2][k], W[3][k]);
  }
  float t[8];
#pragma unroll
  for (int k = 0; k < 8; ++k) {
    const float a0 = dot2(wp[8 + k], xp[0][1], dot2(wp[k], xp[0][0], 0.f)), a1 = dot2(wp[8 + k], xp[1][1], dot2(wp[k], xp[1][0], 0.f));
    const float a2 = dot2(wp[8 + k], xp[2][1], dot2(wp[k], xp[2][0], 0.f)), a3 = dot2(wp[8 + k], xp[3][1], dot2(wp[k], xp[3][0], 0.f));
    t[k] = swap16_add(swap32_add(a0, a2), swap32_add(a1, a3));
  }
  f32x4 o;
#pragma unroll
  for (int k = 0; k < 4; ++k) {
    const float t1 = t[k] + dpp<XOR8>(t[k]), t2 = t[k + 4] + dpp<XOR8>(t[k + 4]);
    o[k] = (c & 8) ? t2 : t1;
  }
  return o;
}

__device__ __forceinline__ void ld_quarter(v4u* R, rsrc_t rk, rsrc_t rv, unsigned voff, int n, int rot, unsigned org) {
  sleep_to(org + sched_of(n), n < 128 ? 512 : 0);
  SCHED_FENCE();
  const rsrc_t rn = n < 64 ? rk : rv;
  const unsigned off = n < 128 ? ((unsigned)(n + rot) & 63u) << 14 : (1u << 20);
#pragma unroll
  for (int i = 0; i < 4; ++i) R[i] = ld16<AUX_NT>(rn, voff + i * 1024, off);
  SCHED_FENCE();
}
#define WAIT_Q() asm volatile("s_waitcnt vmcnt(4)" ::: "memory")

template <bool REFILL, int SKIP = 0>
__device__ __forceinline__ float k_group(v4u* R, const v4u& q, int c, rsrc_t rk, rsrc_t rv, unsigned voff, int n0, int rot, unsigned tb) {
  float x[16];
#pragma unroll
  for (int k = 0; k < 4; ++k) {
    if (REFILL && k >= SKIP) {
      WAIT_Q();
      ld_quarter(R + 4 * ((k + DIST) & 3), rk, rv, voff, n0 + k + DIST, rot, tb);
    }
#pragma unroll
    for (int i = 4 * k; i < 4 * k + 4; ++i) {
      float d = dot2(R[i][0], q[0], 0.f); d = dot2(R[i][1], q[1], d); d = dot2(R[i][2], q[2], d); x[i] = dot2(R[i][3], q[3], d);
    }
    PIN4(x + 4 * k);
    SCHED_FENCE();
  }
#define TR_STEP(HALF, CTRL, BIT)                                                                 \
  _Pragma("unroll") for (int hh_ = 0; hh_ < (HALF); ++hh_) {                                     \
    const float lo_ = x[hh_], hi_ = x[hh_ + (HALF)];                                             \
    const float t1_ = lo_ + dpp<CTRL>(lo_), t2_ = hi_ + dpp<CTRL>(hi_);                          \
    x[hh_] = (c & (BIT)) ? t2_ : t1_;                                                            \
  }
  TR_STEP(8, XOR8, 8) TR_STEP(4, XOR7, 4) TR_STEP(2, XOR2, 2) TR_STEP(1, XOR1, 1)
#undef TR_STEP
  return rbf(rbf(x[0]) * SCALE);
}

template <int I> __device__ __forceinline__ void pv_one(float (&acc)[8], const v4u& V, float p) {
  const float pb = dpp<BCAST + I>(p);
#pragma unroll
  for (int k = 0; k < 4; ++k) {
    acc[2 * k] = fmaf(pb, bflo(V[k]), acc[2 * k]);
    acc[2 * k + 1] = fmaf(pb, bfhi(V[k]), acc[2 * k + 1]);
  }
}

__global__ void __launch_bounds__(512, 2)
fused_decode_kernel(const u16* __restrict__ x, const u16* __restrict__ kc, const u16* __restrict__ vc, const u16* __restrict__ wq,
                    const u16* __restrict__ wo, const u16* __restrict__ wg, const u16* __restrict__ wu, const u16* __restrict__ wd,
                    u16* __restrict__ out, unsigned char* ws) {
  __shared__ __attribute__((aligned(16))) unsigned lds[L_END];
  float* ldsf = reinterpret_cast<float*>(lds);
  const int tid = threadIdx.x, lane = tid & 63;
  const int wave = __builtin_amdgcn_readfirstlane(tid >> 6) ^ 4;
  const int r = lane >> 4, c = lane & 15;
  const int bid = blockIdx.x, xg = bid & 7, idx = bid >> 3;
  const int hh = idx >> 3, h = xg + 8 * hh, b = (idx >> 1) & 3, s = idx & 1;
  const int bh = b * NH + h, u = h * 2 + s;
  const rsrc_t rws = mk_rsrc(ws, WS_END);
  const unsigned tb = now32();

  if (wave < 4) {
    const size_t kvoff = ((size_t)bh * SK + (size_t)s * (SK / 2)) * DH;
    const rsrc_t rk = mk_rsrc(kc + kvoff, 1u << 20), rv = mk_rsrc(vc + kvoff, 1u << 20);
    const unsigned voff = lane * 16 + wave * 4096;
    const int rot = bh * 2 + s;
    const unsigned ph = (unsigned)((((idx + xg) & 7) * TAU256) >> 11);
    const unsigned ta = tb + ph;
    v4u P[32], R[16];
    wg_barrier();
    ld_quarter(P, rk, rv, voff, 0, rot, ta);
    ld_quarter(P + 4, rk, rv, voff, 1, rot, ta);
#pragma unroll
    for (int k = 2; k < 8; ++k) { WAIT_Q(); ld_quarter(P + 4 * k, rk, rv, voff, k, rot, ta); }
#pragma unroll
    for (int k = 0; k < 4; ++k) { WAIT_Q(); ld_quarter(R + 4 * k, rk, rv, voff, 8 + k, rot, ta); }
    wg_barrier();
    v4u q;
    {
      float qs[8];
#pragma unroll
      for (int k = 0; k < 8; ++k) qs[k] = 0.f;
#pragma unroll
      for (int w = 0; w < 4; ++w) {
        const f32x4 a0 = *reinterpret_cast<const f32x4*>(ldsf + L_QP + w * 128 + 8 * c);
        const f32x4 a1 = *reinterpret_cast<const f32x4*>(ldsf + L_QP + w * 128 + 8 * c + 4);
#pragma unroll
        for (int k = 0; k < 4; ++k) { qs[k] += a0[k]; qs[4 + k] += a1[k]; }
      }
#pragma unroll
      for (int k = 0; k < 4; ++k) q[k] = pk_bf16(qs[2 * k], qs[2 * k + 1]);
    }
    const unsigned tag = lds[L_TAG];
    const unsigned tc = __builtin_amdgcn_readfirstlane(lds[L_TAG + 1]) + ph;
    float* sc = ldsf + L_SC + wave * 1024 + lane;
    float mx = k_group<true, 4 - DIST>(R, q, c, rk, rv, voff, 8, rot, tc);
    sc[2 * 64] = mx;
    {
      const float sv = k_group<false>(P, q, c, rk, rv, voff, 0, rot, tc);
      sc[0] = sv;
      mx = fmaxf(mx, sv);
    }
#pragma unroll 1
    for (int g = 3; g < 16; ++g) {
      const float sv = k_group<true>(R, q, c, rk, rv, voff, 4 * g, rot, tc);
      sc[g * 64] = sv;
      mx = fmaxf(mx, sv);
      if (g == 3) {
        const float sw = k_group<false>(P + 16, q, c, rk, rv, voff, 0, rot, tc);
        sc[64] = sw;
        mx = fmaxf(mx, sw);
      }
    }
    mx = wave_max(mx);
    if (lane == 0) ldsf[L_MX + wave] = mx;
    lds_signal(lds + L_FL + 16 + wave, lane);
    float acc[8];
#pragma unroll
    for (int k = 0; k < 8; ++k) acc[k] = 0.f;
#pragma unroll 1
    for (int g = 0; g < 16; ++g) {
      const float p = __builtin_amdgcn_exp2f((sc[g * 64] - mx) * LOG2E);
      if (g == 10 && lane == 0) *(lds_vu32*)(lds + L_FL + 12 + wave) = 1u;
      const int n0 = 64 + 4 * g + DIST;
#define PV_Q(k)                                                                                  \
      WAIT_Q();                                                                                  \
      ld_quarter(R + 4 * (((k) + DIST) & 3), rv, rv, voff, n0 + (k), rot, tc);                   \
      pv_one<4 * (k)>(acc, R[4 * (k)], p); pv_one<4 * (k) + 1>(acc, R[4 * (k) + 1], p);          \
      pv_one<4 * (k) + 2>(acc, R[4 * (k) + 2], p); pv_one<4 * (k) + 3>(acc, R[4 * (k) + 3], p);  \
      PIN8(acc);                                                                                 \
      SCHED_FENCE();
      PV_Q(0) PV_Q(1) PV_Q(2) PV_Q(3)
#undef PV_Q
    }
    {
      float o0, o1;
      rows_reduce8(acc, o0, o1);
      *reinterpret_cast<f32x2*>(ldsf + L_OP + wave * 128 + 8 * c + 2 * r) = f32x2{o0, o1};
    }
    wg_barrier();
    if (wave != 0) return;
    const unsigned t2 = now32();
    const unsigned ooff = (unsigned)(b * DM + 8 * u + 2 * (lane & 3)) * 2u;
    const unsigned xw = __builtin_amdgcn_raw_buffer_load_b32(mk_rsrc(x, 4 * DM * 2), ooff, 0, 0);
    const unsigned ybase = WS_Y + (unsigned)(b * 64 + u) * 4096u;
    wait_until(t2 + YDELAY + (unsigned)((7 * TAU256) >> 11) - ph);
    v4u g0, g1, g2, g3;
#pragma unroll 1
    for (unsigned spin = 0;; ++spin) {
      MEM_FENCE();
      g0 = ld16<AUX_SC1>(rws, lane * 16, ybase);
      g1 = ld16<AUX_SC1>(rws, lane * 16 + 1024, ybase);
      g2 = ld16<AUX_SC1>(rws, lane * 16 + 2048, ybase);
      g3 = ld16<AUX_SC1>(rws, lane * 16 + 3072, ybase);
      const bool ok = g0[1] == tag && g0[3] == tag && g1[1] == tag && g1[3] == tag && g2[1] == tag && g2[3] == tag && g3[1] == tag && g3[3] == tag;
      if (__all(ok) || spin > SPIN_CAP) break;
      __builtin_amdgcn_s_sleep(4);
    }
    float y0 = (__uint_as_float(g0[0]) + __uint_as_float(g1[0])) + (__uint_as_float(g2[0]) + __uint_as_float(g3[0]));
    float y1 = (__uint_as_float(g0[2]) + __uint_as_float(g1[2])) + (__uint_as_float(g2[2]) + __uint_as_float(g3[2]));
    y0 += dpp<ROR4>(y0); y1 += dpp<ROR4>(y1);
    y0 += dpp<XOR8>(y0); y1 += dpp<XOR8>(y1);
    y0 = swap16_add(y0, y0); y1 = swap16_add(y1, y1);
    y0 = swap32_add(y0, y0); y1 = swap32_add(y1, y1);
    __builtin_amdgcn_raw_buffer_store_b32(pk_bf16(bflo(xw) + y0, bfhi(xw) + y1), mk_rsrc(out, 4 * DM * 2), lane < 4 ? ooff : OOR, 0, 0);
    __builtin_amdgcn_raw_buffer_store_b32(tag, rws, (u == 0 && b == 0 && lane == 0) ? WS_EPOCH : OOR, 0, AUX_SC1);
  } else {
    const int hw = wave - 4;
    const v4u xr = ld16<0>(mk_rsrc(x + hw * DM, DM * 2), lane * 16, 0);
    const unsigned epoch = __builtin_amdgcn_raw_buffer_load_b32(rws, 0, WS_EPOCH, AUX_SC1);
    SCHED_FENCE();
    const int cq = xg * 32 + idx, pb = cq >> 2, kq = cq & 3;
    v4u W[32];
    {
      const rsrc_t rq = mk_rsrc(wq + (size_t)(128 * kq + 32 * hw) * (NH * DH) + pb * 64, 31 * 8192 + 128);
#pragma unroll
      for (int i = 0; i < 4; ++i) W[i] = ld16<AUX_NT>(rq, (lane >> 3) * (4 * 8192) + (lane & 7) * 16, i * 8192);
      const u16* wf = pb < 28 ? wg + pb * 64 : wu + (pb < 56 ? pb - 28 : 0) * 64;
      const rsrc_t rf = mk_rsrc(wf + (size_t)(128 * kq + 32 * hw) * FF, pb < 56 ? 31 * FF * 2 + 128 : 0);
#pragma unroll
      for (int i = 0; i < 4; ++i) W[4 + i] = ld16<AUX_NT>(rf, (lane >> 3) * (4 * FF * 2) + (lane & 7) * 16, i * (FF * 2));
    }
    SCHED_FENCE();
    if (hw == 0 && lane < 20) lds[L_FL + lane] = 0u;
    wg_barrier();
    {
      float xv[8];
#pragma unroll
      for (int k = 0; k < 4; ++k) { xv[2 * k] = bflo(xr[k]); xv[2 * k + 1] = bfhi(xr[k]); }
      const float sm = ((xv[0] + xv[1]) + (xv[2] + xv[3])) + ((xv[4] + xv[5]) + (xv[6] + xv[7]));
      const float mean = wave_sum(sm) * (1.f / DM);
      float sq = 0.f;
#pragma unroll
      for (int k = 0; k < 8; ++k) { const float d = xv[k] - mean; sq = fmaf(d, d, sq); }
      const float var = wave_sum(sq) * (1.f / DM);
      const float mb = rbf(mean);
      const float sd = rbf(sqrtf(rbf(rbf(var) + 1e-5f)));
      const float ri = 1.f / sd;
      float xn[8];
#pragma unroll
      for (int k = 0; k < 8; ++k) {
        const float d = rbf(xv[k] - mb);
        const float q0 = d * ri;
        xn[k] = fmaf(fmaf(-q0, sd, d), ri, q0);
      }
      *reinterpret_cast<v4u*>(lds + L_XN + hw * 256 + 4 * lane) =
          v4u{pk_bf16(xn[0], xn[1]), pk_bf16(xn[2], xn[3]), pk_bf16(xn[4], xn[5]), pk_bf16(xn[6], xn[7])};
    }
    const unsigned tag = epoch + TAG_STEP;
    if (hw == 0 && lane == 0) lds[L_TAG] = tag;
    if (hw == 1 && idx == 0)
      __builtin_amdgcn_raw_buffer_store_b64(v2u{tb, tag}, rws, lane < 32 ? WS_T0 + (unsigned)lane * 128u + (unsigned)xg * 8u : OOR, 0, AUX_SC1);
    lds_signal(lds + L_FL + hw, lane);
    lds_wait4(lds + L_FL);
    {
      v2u xp[4];
#pragma unroll
      for (int bb = 0; bb < 4; ++bb) xp[bb] = *reinterpret_cast<const v2u*>(lds + L_XN + bb * 256 + 64 * kq + 16 * hw + 2 * (lane >> 3));
      float* px = ldsf + L_SC + (hw * 4 + r) * 128 + 8 * (c & 7) + 4 * (c >> 3);
      *reinterpret_cast<f32x4*>(px) = proj_part(W, xp, c);
      *reinterpret_cast<f32x4*>(px + 64) = proj_part(W + 4, xp, c);
      lds_signal(lds + L_FL + 4 + hw, lane);
      lds_wait4(lds + L_FL + 4);
      float p0 = 0.f, p1 = 0.f;
#pragma unroll
      for (int w = 0; w < 4; ++w) {
        const f32x2 tt = *reinterpret_cast<const f32x2*>(ldsf + L_SC + (w * 4 + hw) * 128 + 2 * lane);
        p0 += tt[0]; p1 += tt[1];
      }
      const unsigned po = WS_P + (unsigned)(((hw * 120 + (lane < 32 ? pb : 64 + pb)) * 4 + kq) * 512 + (lane & 31) * 16);
      __builtin_amdgcn_raw_buffer_store_b128(v4u{__float_as_uint(p0), tag, __float_as_uint(p1), tag}, rws, (lane < 32 || pb < 56) ? po : OOR, 0, AUX_SC1);
    }
    {
      const unsigned qb = WS_P + (unsigned)(((b * 120 + 2 * h + (lane >> 5)) * 4 + hw) * 512 + (lane & 31) * 16);
      wait_until(tb + QDELAY);
      const v2u t0 = __builtin_amdgcn_raw_buffer_load_b64(rws, (hw == 1 && lane < 8) ? WS_T0 + (unsigned)idx * 128u + (unsigned)lane * 8u : OOR, 0, AUX_SC1);
      const int mat = lane >= 14, piece = lane - 14 * mat, col = 28 * u + 2 * piece;
      const unsigned gb = lane < 28 ? WS_P + (unsigned)(((b * 120 + 64 + 28 * mat + (col >> 6)) * 4 + hw) * 512 + (col & 63) * 8) : OOR;
      v4u pa, gu;
#pragma unroll 1
      for (unsigned spin = 0;; ++spin) {
        MEM_FENCE();
        pa = ld16<AUX_SC1>(rws, qb, 0);
        gu = ld16<AUX_SC1>(rws, gb, 0);
        if (__all(pa[1] == tag && pa[3] == tag && (lane >= 28 || (gu[1] == tag && gu[3] == tag))) || spin > SPIN_CAP) break;
        __builtin_amdgcn_s_sleep(8);
      }
      if (lane < 28) *reinterpret_cast<v2u*>(lds + L_GU + (hw * 2 + mat) * 32 + 2 * piece) = v2u{gu[0], gu[2]};
      if (hw == 1) {
        const bool okt = lane < 8 && t0[1] == tag;
        const float dd = okt ? fminf(fmaxf((float)(int)(t0[0] - tb), -400.f), 400.f) : 0.f;
        float sm = dd, cn = okt ? 1.f : 0.f;
        sm += dpp<XOR1>(sm); sm += dpp<XOR2>(sm); sm += dpp<XOR7>(sm);
        cn += dpp<XOR1>(cn); cn += dpp<XOR2>(cn); cn += dpp<XOR7>(cn);
        if (lane == 0) lds[L_TAG + 1] = tb + (unsigned)(int)(sm / fmaxf(cn, 1.f));
      }
      *reinterpret_cast<v2u*>(lds + L_QP + hw * 128 + 2 * lane) = v2u{pa[0], pa[2]};
    }
    wg_barrier();
    unsigned hp;
    {
      const int j = 8 * r + (c & 7);
      float gg = 0.f, uu = 0.f;
#pragma unroll
      for (int k4 = 0; k4 < 4; ++k4) { gg += ldsf[L_GU + k4 * 64 + j]; uu += ldsf[L_GU + k4 * 64 + 32 + j]; }
      gg = rbf(gg); uu = rbf(uu);
      const float sg = rbf(gg / (1.f + __expf(-gg)));
      const float hv = j < 28 ? sg * uu : 0.f;
      hp = pk_bf16(hv, dpp<XOR1>(hv));
      asm volatile("" : "+v"(hp));
    }
    const unsigned cyc0 = __builtin_amdgcn_readfirstlane(lds[L_TAG + 1]) + WIN_START + (unsigned)(hh * (WIN_PER / 4));
    float y[8];
#pragma unroll
    for (int k = 0; k < 8; ++k) y[k] = 0.f;
    {
      const rsrc_t rd = mk_rsrc(wd + (size_t)(28 * u) * DM + 128 * hw, 27 * 1024 + 256);
#define DSTEP(J)                                                                                 \
      {                                                                                          \
        wait_until(cyc0 + (unsigned)((J) * WIN_PER));                                            \
        const v4u d0 = ld16<AUX_NT>(rd, r * (8 * 1024) + c * 16, (2 * (J)) * 1024);              \
        const v4u d1 = ld16<AUX_NT>(rd, r * (8 * 1024) + c * 16, (2 * (J) + 1) * 1024);          \
        gemv_pair(y, d0, d1, dppu<BCAST + 2 * (J)>(hp));                                         \
        PIN8(y);                                                                                 \
      }
      DSTEP(0) DSTEP(1) DSTEP(2) DSTEP(3)
#undef DSTEP
      const rsrc_t ro = mk_rsrc(wo + (size_t)(h * DH) * DM + 128 * hw, 127 * 1024 + 256);
#pragma unroll
      for (int j = 0; j < 16; ++j) {
        wait_until(cyc0 + (unsigned)((4 + j) * WIN_PER));
        const v4u w0 = ld16<AUX_NT>(ro, r * (32 * 1024) + c * 16, (2 * j) * 1024);
        const v4u w1 = ld16<AUX_NT>(ro, r * (32 * 1024) + c * 16, (2 * j + 1) * 1024);
#pragma unroll
        for (int k = 0; k < 4; ++k) { W[2 * j][k] = perm_lo(w0[k], w1[k]); W[2 * j + 1][k] = perm_hi(w0[k], w1[k]); }
        asm volatile("" : "+v"(W[2 * j]), "+v"(W[2 * j + 1]) :: "memory");
        if (j == 8) {
#pragma unroll 1
          for (unsigned spin = 0; spin < SPIN_CAP; ++spin) {
            if (*(lds_vu32*)(lds + L_FL + 16 + hw) != 0u) break;
            __builtin_amdgcn_s_sleep(16);
          }
          MEM_FENCE();
          const float mw = ldsf[L_MX + hw];
          float l = 0.f;
#pragma unroll
          for (int g = 0; g < 16; ++g) l += __builtin_amdgcn_exp2f((ldsf[L_SC + hw * 1024 + g * 64 + lane] - mw) * LOG2E);
          l = wave_sum(l);
          __builtin_amdgcn_raw_buffer_store_b128(v4u{__float_as_uint(mw), tag, __float_as_uint(l), tag}, rws,
                                                 lane == 0 ? WS_ML + (unsigned)((bh * 2 + s) * 4 + hw) * 16u : OOR, 0, AUX_SC1);
        }
      }
    }
    float f[4];
    {
#pragma unroll 1
      for (unsigned spin = 0; spin < SPIN_CAP; ++spin) {
        if (*(lds_vu32*)(lds + L_FL + 12 + hw) != 0u) break;
        __builtin_amdgcn_s_sleep(32);
      }
      const unsigned o = lane < 8 ? WS_ML + (unsigned)(bh * 8 + lane) * 16u : OOR;
      v4u ml;
#pragma unroll 1
      for (unsigned spin = 0;; ++spin) {
        MEM_FENCE();
        ml = ld16<AUX_SC1>(rws, o, 0);
        if (__all(lane >= 8 || (ml[1] == tag && ml[3] == tag)) || spin > SPIN_CAP) break;
        __builtin_amdgcn_s_sleep(64);
      }
      const float mw = lane < 8 ? __uint_as_float(ml[0]) : -INFINITY;
      float M = fmaxf(mw, dpp<XOR1>(mw)); M = fmaxf(M, dpp<XOR2>(M)); M = fmaxf(M, dpp<XOR7>(M));
      const float wt = lane < 8 ? __builtin_amdgcn_exp2f((mw - M) * LOG2E) : 0.f;
      float L = wt * (lane < 8 ? __uint_as_float(ml[2]) : 0.f);
      L += dpp<XOR1>(L); L += dpp<XOR2>(L); L += dpp<XOR7>(L);
      const float fw = wt / L;
#pragma unroll
      for (int w = 0; w < 4; ++w) f[w] = __uint_as_float(__builtin_amdgcn_readlane(__float_as_uint(fw), 4 * s + w));
      PIN8(y);
    }
    wg_barrier();
    {
      float o0 = 0.f, o1 = 0.f;
#pragma unroll
      for (int w = 0; w < 4; ++w) {
        const f32x2 t = *reinterpret_cast<const f32x2*>(ldsf + L_OP + w * 128 + 32 * r + 2 * c);
        o0 = fmaf(f[w], t[0], o0); o1 = fmaf(f[w], t[1], o1);
      }
      const unsigned opk = pk_bf16(o0, o1);
#define YGEMV(J)                                                                                 \
  {                                                                                              \
    const unsigned ob = dppu<BCAST + (J)>(opk);                                                  \
    _Pragma("unroll") for (int k = 0; k < 4; ++k) {                                              \
      y[2 * k] = dot2(W[2 * (J)][k], ob, y[2 * k]);                                              \
      y[2 * k + 1] = dot2(W[2 * (J) + 1][k], ob, y[2 * k + 1]);                                  \
    }                                                                                            \
  }
      YGEMV(0) YGEMV(1) YGEMV(2) YGEMV(3) YGEMV(4) YGEMV(5) YGEMV(6) YGEMV(7)
      YGEMV(8) YGEMV(9) YGEMV(10) YGEMV(11) YGEMV(12) YGEMV(13) YGEMV(14) YGEMV(15)
#undef YGEMV
      float y0, y1;
      rows_reduce8(y, y0, y1);
      const unsigned o = WS_Y + (unsigned)(b * 64 + 16 * hw + c) * 4096u + (unsigned)u * 64u + (unsigned)r * 16u;
      __builtin_amdgcn_raw_buffer_store_b128(v4u{__float_as_uint(y0), tag, __float_as_uint(y1), tag}, rws, o, 0, AUX_SC1);
    }
  }
}

}

extern "C" void kernel_launch(void* const* d_in, const int* in_sizes, int n_in, void* d_out, int out_size, void* d_ws, size_t ws_size,
                              hipStream_t stream) {
  (void)in_sizes; (void)n_in; (void)out_size; (void)ws_size;
  hipLaunchKernelGGL(fused_decode_kernel, dim3(256), dim3(512), 0, stream, (const u16*)d_in[0], (const u16*)d_in[1], (const u16*)d_in[2],
                     (const u16*)d_in[3], (const u16*)d_in[4], (const u16*)d_in[6], (const u16*)d_in[7], (const u16*)d_in[8], (u16*)d_out,
                     (unsigned char*)d_ws);
}
```
